```python
import torch

def _softmax(x, axis=-1):
    return torch.softmax(x, dim=axis)
def _gelu(x):
    return torch.nn.functional.gelu(x, approximate="tanh")
def _silu(x):
    return torch.nn.functional.silu(x)
def _sigmoid(x):
    return torch.sigmoid(x)
def _layernorm(x, axis=-1, eps=1e-5):
    m = x.mean(dim=axis, keepdim=True)
    v = x.var(dim=axis, keepdim=True, unbiased=False)
    return (x - m) / torch.sqrt(v + eps)
def _scatter_add(tgt, idx, src, axis=0):
    return tgt.clone().index_add_(axis, idx, src)
def _segment_sum(src, seg, n):
    out = torch.zeros((n,) + src.shape[1:], dtype=src.dtype)
    return out.index_add_(0, seg, src)
def _pad(x, pw):
    flat = tuple(v for pair in reversed(pw) for v in pair)
    return torch.nn.functional.pad(x, flat)

kernel_name = 'recurrent_scan__ssm_conv_s4d__dense_activation_relu__prompt'

def setup_inputs():

    _w = (lambda a: (lambda t: t.to(torch.bfloat16) if t.is_floating_point() else t)(torch.as_tensor(a)))
    def _A_setup():
        Bsz, L, D, N = 1, 2048, 448, 256
        return {
            "u": _w((lambda *s: torch.randn(*s, dtype=torch.bfloat16) * 0.05)(Bsz, L, D)),
            "log_A_real": _w((lambda *s: torch.randn(*s, dtype=torch.bfloat16) * 6.4)(D, N) * 0.5),
            "A_imag": _w((lambda *s: torch.randn(*s, dtype=torch.bfloat16) * 0.05)(D, N) * 3.14),
            "C_real": _w((lambda *s: torch.randn(*s, dtype=torch.bfloat16) * 0.05)(D, N) / N ** 0.5),
            "C_imag": _w((lambda *s: torch.randn(*s, dtype=torch.bfloat16) * 0.05)(D, N) / N ** 0.5),
            "log_dt": _w((lambda *s: torch.randn(*s, dtype=torch.bfloat16) * 0.05)(D) * 0.5 - 4.0),
            "D_skip": _w((lambda *s: torch.randn(*s, dtype=torch.bfloat16) * 0.05)(D)),
        }
    _A_in = _A_setup()

    def _B_setup():
        B, S, D, Dff = 1, 2048, 448, 1792
        return {
            "x": _w((lambda *s: torch.randn(*s, dtype=torch.bfloat16) * 0.05)(B, S, D)),
            "w1": _w((lambda *s: torch.randn(*s, dtype=torch.bfloat16) * 0.05)(D, Dff)),
            "w2": _w((lambda *s: torch.randn(*s, dtype=torch.bfloat16) * 0.05)(Dff, D)),
        }
    _B_in = _B_setup()

    return {
        "A_u": _A_in["u"],
        "A_log_A_real": _A_in["log_A_real"],
        "A_A_imag": _A_in["A_imag"],
        "A_C_real": _A_in["C_real"],
        "A_C_imag": _A_in["C_imag"],
        "A_log_dt": _A_in["log_dt"],
        "A_D_skip": _A_in["D_skip"],
        "B_x": _B_in["x"],
        "B_w1": _B_in["w1"],
        "B_w2": _B_in["w2"],
    }

def reference(A_u, A_log_A_real, A_A_imag, A_C_real, A_C_imag, A_log_dt, A_D_skip, B_x, B_w1, B_w2):
    def _A_ref(u, log_A_real, A_imag, C_real, C_imag, log_dt, D_skip):
        L = u.shape[1]
        dt = torch.exp(log_dt)
        A = -torch.exp(log_A_real) + 1j * A_imag
        C = (C_real + 1j * C_imag) * dt[:, None]
        pos = (lambda x, dt: x.to(dt))(torch.arange(L), log_dt.dtype)
        P = torch.exp((A * dt[:, None])[:, :, None] * pos[None, None, :])
        K = 2.0 * torch.einsum("dn,dnl->dl", C, P).real
        L2 = 2 * L
        u_t = (lambda x, *d: x.permute(*d))(u, 0, 2, 1)
        Kf = (lambda x, *a, **k: torch.fft.rfft(x.float(), *a, **k))(K, L2)
        Uf = (lambda x, *a, **k: torch.fft.rfft(x.float(), *a, **k))(u_t, L2)
        y = (lambda x, *a, **k: torch.fft.irfft(x, *a, **k).to(torch.bfloat16))(Uf * Kf[None, :, :], L2)[:, :, :L]
        y = (lambda x, *d: x.permute(*d))(y, 0, 2, 1)
        return y + u * D_skip[None, None, :]

    def _B_ref(x, w1, w2):
        h = torch.einsum("bsd,df->bsf", x, w1)
        zero = torch.zeros_like(h)
        h = torch.maximum(h, zero)
        return torch.einsum("bsf,fd->bsd", h, w2)

    x = A_u
    xn = _layernorm(x, axis=-1)
    s = _A_ref(xn, A_log_A_real, A_A_imag, A_C_real, A_C_imag, A_log_dt, A_D_skip)
    g = _silu(_B_ref(xn, B_w1, B_w2))
    return x + s * g
```

```cpp
#include <hip/hip_runtime.h>
#include <hip/hip_bf16.h>
#include <stdint.h>

#define L_ 2048
#define D_ 448
#define N_ 256
#define F_ 1792

__device__ __forceinline__ float bf2f(uint16_t v) { return __uint_as_float(((uint32_t)v) << 16); }
__device__ __forceinline__ uint16_t f2bf(float f) {
    uint32_t u = __float_as_uint(f);
    u += 0x7FFFu + ((u >> 16) & 1u);
    return (uint16_t)(u >> 16);
}
__device__ __forceinline__ float rbf(float f) { return bf2f(f2bf(f)); }

__global__ void ln_kernel(const uint16_t* __restrict__ u, uint16_t* __restrict__ xn) {
    int row = blockIdx.x;
    int lane = threadIdx.x;
    const uint16_t* ur = u + (size_t)row * D_;
    float v[7];
    float s = 0.f;
    for (int i = 0; i < 7; ++i) { v[i] = bf2f(ur[lane + 64 * i]); s += v[i]; }
    for (int m = 32; m >= 1; m >>= 1) s += __shfl_xor(s, m);
    float mean = s / (float)D_;
    float q = 0.f;
    for (int i = 0; i < 7; ++i) { float d = v[i] - mean; q += d * d; }
    for (int m = 32; m >= 1; m >>= 1) q += __shfl_xor(q, m);
    float var = q / (float)D_;
    float mb = rbf(mean);
    float vb = rbf(var);
    float sd = rbf(sqrtf(rbf(vb + 1e-5f)));
    for (int i = 0; i < 7; ++i) {
        float a = rbf(v[i] - mb);
        xn[(size_t)row * D_ + lane + 64 * i] = f2bf(a / sd);
    }
}

__global__ void kgen_kernel(const uint16_t* __restrict__ lar, const uint16_t* __restrict__ aim,
                            const uint16_t* __restrict__ cr, const uint16_t* __restrict__ ci,
                            const uint16_t* __restrict__ ldt, float* __restrict__ K) {
    int d = blockIdx.x;
    __shared__ float sar[N_], sai[N_], scr[N_], sci[N_];
    float dt = rbf(expf(bf2f(ldt[d])));
    for (int n = threadIdx.x; n < N_; n += blockDim.x) {
        float ar = -rbf(expf(bf2f(lar[d * N_ + n])));
        sar[n] = ar * dt;
        sai[n] = bf2f(aim[d * N_ + n]) * dt;
        scr[n] = bf2f(cr[d * N_ + n]) * dt;
        sci[n] = bf2f(ci[d * N_ + n]) * dt;
    }
    __syncthreads();
    for (int l = threadIdx.x; l < L_; l += blockDim.x) {
        float pos = rbf((float)l);
        float acc = 0.f;
        for (int n = 0; n < N_; ++n) {
            float e = expf(sar[n] * pos);
            float s, c;
            sincosf(sai[n] * pos, &s, &c);
            acc += scr[n] * (e * c) - sci[n] * (e * s);
        }
        K[(size_t)d * L_ + l] = 2.f * acc;
    }
}

__global__ void conv_kernel(const float* __restrict__ K, const uint16_t* __restrict__ xn,
                            const uint16_t* __restrict__ dskip, uint16_t* __restrict__ s) {
    int d = blockIdx.x;
    __shared__ float sk[L_], sx[L_];
    for (int l = threadIdx.x; l < L_; l += blockDim.x) { sk[l] = K[(size_t)d * L_ + l]; sx[l] = bf2f(xn[(size_t)l * D_ + d]); }
    __syncthreads();
    float ds = bf2f(dskip[d]);
    for (int t = threadIdx.x; t < L_; t += blockDim.x) {
        float acc = 0.f;
        for (int j = 0; j <= t; ++j) acc += sk[j] * sx[t - j];
        float y = rbf(acc);
        float ud = rbf(sx[t] * ds);
        s[(size_t)t * D_ + d] = f2bf(y + ud);
    }
}

__global__ void gemm1_kernel(const uint16_t* __restrict__ xn, const uint16_t* __restrict__ w1, uint16_t* __restrict__ h) {
    int t = blockIdx.x;
    __shared__ float sx[D_];
    for (int i = threadIdx.x; i < D_; i += blockDim.x) sx[i] = bf2f(xn[(size_t)t * D_ + i]);
    __syncthreads();
    for (int f = threadIdx.x; f < F_; f += blockDim.x) {
        float acc = 0.f;
        for (int k = 0; k < D_; ++k) acc += sx[k] * bf2f(w1[(size_t)k * F_ + f]);
        float hv = rbf(acc);
        h[(size_t)t * F_ + f] = f2bf(fmaxf(hv, 0.f));
    }
}

__global__ void gemm2_kernel(const uint16_t* __restrict__ h, const uint16_t* __restrict__ w2,
                             const uint16_t* __restrict__ s, const uint16_t* __restrict__ u, uint16_t* __restrict__ out) {
    int t = blockIdx.x;
    __shared__ float sh[F_];
    for (int i = threadIdx.x; i < F_; i += blockDim.x) sh[i] = bf2f(h[(size_t)t * F_ + i]);
    __syncthreads();
    for (int d = threadIdx.x; d < D_; d += blockDim.x) {
        float acc = 0.f;
        for (int k = 0; k < F_; ++k) acc += sh[k] * bf2f(w2[(size_t)k * D_ + d]);
        float o = rbf(acc);
        float g = rbf(o / (1.f + expf(-o)));
        float sg = rbf(bf2f(s[(size_t)t * D_ + d]) * g);
        out[(size_t)t * D_ + d] = f2bf(bf2f(u[(size_t)t * D_ + d]) + sg);
    }
}

extern "C" void kernel_launch(void* const* d_in, const int* in_sizes, int n_in,
                              void* d_out, int out_size, void* d_ws, size_t ws_size,
                              hipStream_t stream) {
    const uint16_t* u = (const uint16_t*)d_in[0];
    const uint16_t* lar = (const uint16_t*)d_in[1];
    const uint16_t* aim = (const uint16_t*)d_in[2];
    const uint16_t* cr = (const uint16_t*)d_in[3];
    const uint16_t* ci = (const uint16_t*)d_in[4];
    const uint16_t* ldt = (const uint16_t*)d_in[5];
    const uint16_t* dsk = (const uint16_t*)d_in[6];
    const uint16_t* w1 = (const uint16_t*)d_in[8];
    const uint16_t* w2 = (const uint16_t*)d_in[9];
    char* ws = (char*)d_ws;
    uint16_t* xn = (uint16_t*)(ws);
    float* K = (float*)(ws + (2u << 20));
    uint16_t* s = (uint16_t*)(ws + (6u << 20));
    uint16_t* h = (uint16_t*)(ws + (8u << 20));
    ln_kernel<<<L_, 64, 0, stream>>>(u, xn);
    kgen_kernel<<<D_, 256, 0, stream>>>(lar, aim, cr, ci, ldt, K);
    conv_kernel<<<D_, 256, 0, stream>>>(K, xn, dsk, s);
    gemm1_kernel<<<L_, 256, 0, stream>>>(xn, w1, h);
    gemm2_kernel<<<L_, 256, 0, stream>>>(h, w2, s, u, (uint16_t*)d_out);
}
```

```cpp
#include <hip/hip_runtime.h>
#include <stdint.h>

#define L_ 2048
#define D_ 448
#define N_ 256
#define F_ 1792

typedef __attribute__((ext_vector_type(8))) __bf16 bf8;
typedef __attribute__((ext_vector_type(8))) _Float16 h8;
typedef __attribute__((ext_vector_type(4))) short s4v;
typedef __attribute__((ext_vector_type(4))) float f4;
typedef __attribute__((ext_vector_type(2))) float f2;
typedef __attribute__((ext_vector_type(4))) unsigned u4;
typedef __attribute__((ext_vector_type(2))) unsigned u2;
typedef __attribute__((ext_vector_type(2))) __bf16 bf2v;
typedef __attribute__((ext_vector_type(2))) _Float16 h2v;
typedef unsigned long long u64;
#define LAS __attribute__((address_space(3)))
#define GAS __attribute__((address_space(1)))
typedef GAS u64 gu64;
#define RLX_AGENT __ATOMIC_RELAXED, __HIP_MEMORY_SCOPE_AGENT

__device__ __forceinline__ float bf2f(uint16_t v) { return __uint_as_float(((uint32_t)v) << 16); }
__device__ __forceinline__ unsigned pk_bf16(float a, float b) { bf2v r; r.x = (__bf16)a; r.y = (__bf16)b; return __builtin_bit_cast(unsigned, r); }
__device__ __forceinline__ unsigned pk_f16(float a, float b) { h2v r; r.x = (_Float16)a; r.y = (_Float16)b; return __builtin_bit_cast(unsigned, r); }
__device__ __forceinline__ float rbf(float f) { return __uint_as_float(pk_bf16(f, 0.f) << 16); }
__device__ __forceinline__ float lo_f(unsigned w) { return __uint_as_float(w << 16); }
__device__ __forceinline__ float hi_f(unsigned w) { return __uint_as_float(w & 0xffff0000u); }
__device__ __forceinline__ float lo_h(unsigned w) { return (float)__builtin_bit_cast(h2v, w).x; }
__device__ __forceinline__ float hi_h(unsigned w) { return (float)__builtin_bit_cast(h2v, w).y; }
__device__ __forceinline__ f4 mfma_bf(u4 a, u4 b, f4 c) { return __builtin_amdgcn_mfma_f32_16x16x32_bf16(__builtin_bit_cast(bf8, a), __builtin_bit_cast(bf8, b), c, 0, 0, 0); }
__device__ __forceinline__ f4 mfma_h(u4 a, u4 b, f4 c) { return __builtin_amdgcn_mfma_f32_16x16x32_f16(__builtin_bit_cast(h8, a), __builtin_bit_cast(h8, b), c, 0, 0, 0); }
template <int CTRL> __device__ __forceinline__ float dpp_f(float v) { return __int_as_float(__builtin_amdgcn_update_dpp(0, __float_as_int(v), CTRL, 0xf, 0xf, false)); }
__device__ __forceinline__ float row16_sum(float v) { v += dpp_f<0x128>(v); v += dpp_f<0x124>(v); v += dpp_f<0x122>(v); v += dpp_f<0x121>(v); return v; }
__device__ __forceinline__ float row16_max(float v) { v = fmaxf(v, dpp_f<0x128>(v)); v = fmaxf(v, dpp_f<0x124>(v)); v = fmaxf(v, dpp_f<0x122>(v)); v = fmaxf(v, dpp_f<0x121>(v)); return v; }
__device__ __forceinline__ u2 tr1(const char* p) { return __builtin_bit_cast(u2, __builtin_amdgcn_ds_read_tr16_b64_v4i16((LAS s4v*)p)); }
__device__ __forceinline__ void dma16(unsigned voff, const void* sbase, unsigned lds_dst) {
    unsigned keep;
    asm volatile("s_mov_b32 %0, m0\n\ts_mov_b32 m0, %3\n\ts_nop 0\n\tglobal_load_lds_dwordx4 %1, %2\n\ts_mov_b32 m0, %0" : "=&s"(keep) : "v"(voff), "s"(sbase), "s"(lds_dst) : "memory");
}
__device__ __forceinline__ void dma_pair(unsigned dst, unsigned va, unsigned vb, const void* sbase) {
    asm volatile("s_mov_b32 m0, %0\n\ts_nop 0\n\tglobal_load_lds_dwordx4 %1, %3\n\tglobal_load_lds_dwordx4 %2, %3 offset:1024" :: "s"(dst), "v"(va), "v"(vb), "s"(sbase) : "memory");
}
#define WAIT_VM(n) asm volatile("s_waitcnt vmcnt(" #n ")" ::: "memory")
#define WAIT_LGKM0() asm volatile("s_waitcnt lgkmcnt(0)" ::: "memory")
#define RAW_BAR() do { WAIT_LGKM0(); __builtin_amdgcn_s_barrier(); asm volatile("" ::: "memory"); } while (0)

#define TRDECL
#define TR(i)
#define TRFLUSH
#define TRW 0

#define NIT 14
#define SLEEPN 8
#define WS_EPOCH 0
#define WS_FX    4096
#define WS_FS    8192
#define WS_FP    12288
#define WS_PSC   16384
#define WS_XT    (1u << 20)
#define WS_S     (3u << 20)
#define WS_PART  (5u << 20)
#define WS_BYTES (16u << 20)
#define LDS_BYTES 81920
#define SPIN_CAP (1u << 14)

__device__ __forceinline__ void wait_flags(const gu64* p, bool active, u64 tag) {
    for (unsigned spins = 0; spins < SPIN_CAP; ++spins) {
        u64 v = active ? __hip_atomic_load(p, RLX_AGENT) : tag;
        if (__all(v == tag)) break;
        __builtin_amdgcn_s_sleep(SLEEPN);
    }
    __builtin_amdgcn_fence(__ATOMIC_ACQUIRE, "wavefront");
}

#define XH_BYTES 28672
#define RING_W 6144
__device__ __forceinline__ void ffn_role(const uint16_t* __restrict__ u, const uint16_t* __restrict__ w1, const uint16_t* __restrict__ w2,
                                         char* __restrict__ ws, uint16_t* __restrict__ out, char* lds, int b) {
    const int tid = threadIdx.x, lane = tid & 63, wave = __builtin_amdgcn_readfirstlane(tid >> 6);
    const int r16 = lane & 15, g = lane >> 4;
    const int xcd = b & 7, ks = xcd & 3, rt = 32 * (xcd >> 2) + (b >> 3);
    const int rot = (b >> 3) % 14;
    TRDECL;
    TR(0);
    const __amdgpu_buffer_rsrc_t rs = __builtin_amdgcn_make_buffer_rsrc(ws, 0, WS_BYTES, 0x00020000);
    const int lrow = 4 * wave + g;
    u4 ur[4];
    {
        const u4* up = (const u4*)(u + (size_t)(32 * rt + lrow) * D_) + (r16 < 14 ? r16 : 13);
#pragma unroll
        for (int i = 0; i < 4; ++i) ur[i] = up[14 * i];
    }
    const unsigned ldsbase = (unsigned)(size_t)(LAS char*)lds;
    const unsigned ring = ldsbase + XH_BYTES + wave * RING_W;
    const int rho = lane >> 3;
    const unsigned srcc = (unsigned)(((lane & 7) ^ (((rho >> 1) & 3) << 1)) * 16);
    const int krow = 8 * (rho >> 2) + (rho & 3);
    const unsigned v1a = krow * (F_ * 2) + srcc, v1b = v1a + 16 * F_ * 2 - 1024;
    const unsigned v2a = krow * (D_ * 2) + srcc, v2b = v2a + 16 * D_ * 2 - 1024;
    const char* w1b = (const char*)(w1 + 448 * ks + 64 * wave);
    const char* w2b = (const char*)(w2 + (size_t)448 * ks * D_ + 64 * wave);
#define KROT(i) ((i) + rot >= 14 ? (i) + rot - 14 : (i) + rot)
#define ISSUE(s) do { if ((s) < 28) dma_pair(ring + ((s) % 3) * 2048, v1a, v1b, w1b + (size_t)KROT((s) >> 1) * (32 * F_ * 2) + ((s) & 1) * (4 * F_ * 2)); \
                      else if ((s) < 56) dma_pair(ring + ((s) % 3) * 2048, v2a, v2b, w2b + (size_t)KROT(((s) - 28) >> 1) * (32 * D_ * 2) + ((s) & 1) * (4 * D_ * 2)); } while (0)
    u64 tag = 0;
    if (wave < 7) { ISSUE(0); ISSUE(1); ISSUE(2); }
    else tag = __hip_atomic_load((gu64*)(ws + WS_EPOCH), RLX_AGENT) + 1ull;
    {
        const bool act = r16 < 14;
        float s = 0.f;
#pragma unroll
        for (int i = 0; i < 4; ++i)
#pragma unroll
            for (int e = 0; e < 4; ++e) s += lo_f(ur[i][e]) + hi_f(ur[i][e]);
        s = row16_sum(act ? s : 0.f);
        const float mean = s * (1.0f / D_);
        float q = 0.f;
#pragma unroll
        for (int i = 0; i < 4; ++i)
#pragma unroll
            for (int e = 0; e < 4; ++e) { float a = lo_f(ur[i][e]) - mean, c = hi_f(ur[i][e]) - mean; q = fmaf(a, a, q); q = fmaf(c, c, q); }
        q = row16_sum(act ? q : 0.f);
        const float mb = rbf(mean);
        const float sd = rbf(sqrtf(rbf(rbf(q * (1.0f / D_)) + 1e-5f)));
        const float rinv = 1.0f / sd;
        char* xr = lds + lrow * 896;
        const int sw = (lrow >> 1) & 7;
#pragma unroll
        for (int i = 0; i < 4; ++i) {
            u4 o;
#pragma unroll
            for (int e = 0; e < 4; ++e) { unsigned w = ur[i][e]; unsigned a = pk_bf16(lo_f(w) - mb, hi_f(w) - mb); o[e] = pk_bf16(lo_f(a) * rinv, hi_f(a) * rinv); }
            if (act) *(u4*)(xr + 16 * ((r16 + 14 * i) ^ sw)) = o;
        }
    }
    TR(1);
    RAW_BAR();
    TR(2);
    const int q4 = r16 >> 2, p4 = lane & 3;
    if (wave == 7) {
#pragma unroll
        for (int m = 0; m < 2; ++m) {
            const int row = 16 * m + 4 * g + q4;
            const char* xr = lds + row * 896 + (p4 & 1) * 8;
            const int sw = (row >> 1) & 7;
#pragma unroll
            for (int ct = 0; ct < 7; ++ct) {
                u2 v = tr1(xr + 16 * ((2 * (7 * ks + ct) + (p4 >> 1)) ^ sw));
                u2 o = {pk_f16(lo_f(v.x), hi_f(v.x)), pk_f16(lo_f(v.y), hi_f(v.y))};
                __builtin_amdgcn_raw_buffer_store_b64(o, rs, WS_XT + ((2 * rt + m) * 28 + 7 * ks + ct) * 512 + lane * 8, 0, 16);
            }
        }
        WAIT_VM(0);
        if (lane == 0) __hip_atomic_store((gu64*)(ws + WS_FX) + ks * 64 + rt, tag, RLX_AGENT);
        RAW_BAR();
        RAW_BAR();
        wait_flags((const gu64*)(ws + WS_FS) + 8 * (4 * (lane >> 3) + ks) + (lane & 7), lane < 56, tag);
        RAW_BAR();
        if (lane == 0) __hip_atomic_store((gu64*)(ws + WS_FP) + rt * 4 + ks, tag, RLX_AGENT);
        wait_flags((const gu64*)(ws + WS_FP) + rt * 4 + (lane & 3), lane < 4, tag);
        RAW_BAR();
        if (b == 0) {
#pragma unroll
            for (int i = 0; i < 4; ++i) wait_flags((const gu64*)(ws + WS_FS) + 64 * i + lane, 64 * i + lane < 224, tag);
            if (lane == 0) __hip_atomic_store((gu64*)(ws + WS_EPOCH), tag, RLX_AGENT);
        }
        return;
    }
    const int rr = 4 * g + q4;
    const int wtr = rr * 128 + (((rr >> 1) & 3) << 5) + 8 * p4;
    const char* rpb = lds + XH_BYTES + wave * RING_W;
    const char* rp0 = rpb + wtr; const char* rp1 = rpb + (wtr ^ 32); const char* rp2 = rpb + (wtr ^ 64); const char* rp3 = rpb + (wtr ^ 96);
    const int asw = (r16 >> 1) & 7;
    const unsigned ae = (unsigned)((g ^ asw) * 16);
    const char* a0p = lds + r16 * 896;
    f4 acc[2][4];
#pragma unroll
    for (int m = 0; m < 2; ++m)
#pragma unroll
        for (int n = 0; n < 4; ++n) acc[m][n] = (f4){0, 0, 0, 0};
#pragma unroll
    for (int it = 0; it < NIT; ++it) {
        const char* ap = a0p + (ae ^ (unsigned)(KROT(it) << 6));
        const u4 a0 = *(const u4*)ap;
        const u4 a1 = *(const u4*)(ap + 16 * 896);
        u2 x[2][4];
#pragma unroll
        for (int hf = 0; hf < 2; ++hf) {
            const int sl = 2 * it + hf, so = (sl % 3) * 2048;
            WAIT_VM(4);
            x[hf][0] = tr1(rp0 + so); x[hf][1] = tr1(rp1 + so); x[hf][2] = tr1(rp2 + so); x[hf][3] = tr1(rp3 + so);
            WAIT_LGKM0();
            ISSUE(sl + 3);
        }
#pragma unroll
        for (int n = 0; n < 4; ++n) {
            const u4 wf = {x[0][n].x, x[0][n].y, x[1][n].x, x[1][n].y};
            acc[0][n] = mfma_bf(wf, a0, acc[0][n]); acc[1][n] = mfma_bf(wf, a1, acc[1][n]);
        }
    }
    TR(3);
    RAW_BAR();
#pragma unroll
    for (int m = 0; m < 2; ++m)
#pragma unroll
        for (int n = 0; n < 4; ++n) {
            u2 o = {pk_bf16(fmaxf(acc[m][n][0], 0.f), fmaxf(acc[m][n][1], 0.f)), pk_bf16(fmaxf(acc[m][n][2], 0.f), fmaxf(acc[m][n][3], 0.f))};
            *(u2*)(lds + (16 * m + r16) * 896 + 16 * ((8 * wave + 2 * n + (g >> 1)) ^ asw) + 8 * (g & 1)) = o;
            acc[m][n] = (f4){0, 0, 0, 0};
        }
    RAW_BAR();
    TR(4);
#pragma unroll
    for (int it = 0; it < NIT; ++it) {
        const char* ap = a0p + (ae ^ (unsigned)(KROT(it) << 6));
        const u4 a0 = *(const u4*)ap;
        const u4 a1 = *(const u4*)(ap + 16 * 896);
        u2 x[2][4];
#pragma unroll
        for (int hf = 0; hf < 2; ++hf) {
            const int sl = 28 + 2 * it + hf, so = (sl % 3) * 2048;
            if (sl < 54) WAIT_VM(4); else if (sl == 54) WAIT_VM(2); else WAIT_VM(0);
            x[hf][0] = tr1(rp0 + so); x[hf][1] = tr1(rp1 + so); x[hf][2] = tr1(rp2 + so); x[hf][3] = tr1(rp3 + so);
            if (sl + 3 < 56) { WAIT_LGKM0(); ISSUE(sl + 3); }
        }
#pragma unroll
        for (int n = 0; n < 4; ++n) {
            const u4 wf = {x[0][n].x, x[0][n].y, x[1][n].x, x[1][n].y};
            acc[0][n] = mfma_bf(a0, wf, acc[0][n]); acc[1][n] = mfma_bf(a1, wf, acc[1][n]);
        }
    }
    TR(5);
    {
        float mx = 0.f;
#pragma unroll
        for (int m = 0; m < 2; ++m)
#pragma unroll
            for (int n = 0; n < 4; ++n)
#pragma unroll
                for (int e = 0; e < 4; ++e) mx = fmaxf(mx, fabsf(acc[m][n][e]));
        mx = row16_max(mx);
        mx = fmaxf(mx, __shfl_xor(mx, 16)); mx = fmaxf(mx, __shfl_xor(mx, 32));
        int e2 = (int)((__float_as_uint(mx) >> 23) & 255u) - 126;
        e2 = e2 < -100 ? -100 : e2;
        const float sc = __uint_as_float((unsigned)(127 + 15 - e2) << 23);
        const float scinv = __uint_as_float((unsigned)(127 - 15 + e2) << 23);
        const unsigned pbase = WS_PART + (unsigned)(((rt * 4 + ks) * 7 + wave) * 4) * 1024 + lane * 16;
#pragma unroll
        for (int n = 0; n < 4; ++n) {
            if (n != ks) {
                u4 o = {pk_f16(acc[0][n][0] * sc, acc[0][n][1] * sc), pk_f16(acc[0][n][2] * sc, acc[0][n][3] * sc),
                        pk_f16(acc[1][n][0] * sc, acc[1][n][1] * sc), pk_f16(acc[1][n][2] * sc, acc[1][n][3] * sc)};
                __builtin_amdgcn_raw_buffer_store_b128(o, rs, pbase + n * 1024, 0, 16);
            }
        }
        if (lane == 0) __builtin_amdgcn_raw_buffer_store_b32(__float_as_uint(scinv), rs, WS_PSC + ((rt * 4 + ks) * 8 + wave) * 4, 0, 16);
    }
    const int cc = 64 * wave + 16 * ks + r16;
    uint16_t uu[2][4];
#pragma unroll
    for (int m = 0; m < 2; ++m)
#pragma unroll
        for (int e = 0; e < 4; ++e) uu[m][e] = u[(size_t)(32 * rt + 16 * m + 4 * g + e) * D_ + cc];
    f4 own0, own1;
    own0 = ks == 0 ? acc[0][0] : ks == 1 ? acc[0][1] : ks == 2 ? acc[0][2] : acc[0][3];
    own1 = ks == 0 ? acc[1][0] : ks == 1 ? acc[1][1] : ks == 2 ? acc[1][2] : acc[1][3];
    WAIT_VM(0);
    TR(6);
    RAW_BAR();
    RAW_BAR();
    TR(7);
    {
        u4 pv[3]; float psc[3];
#pragma unroll
        for (int j = 0; j < 3; ++j) {
            const int kp = (ks + 1 + j) & 3;
            pv[j] = __builtin_amdgcn_raw_buffer_load_b128(rs, WS_PART + (unsigned)((((rt * 4 + kp) * 7 + wave) * 4) + ks) * 1024 + lane * 16, 0, 16);
            psc[j] = __uint_as_float(__builtin_amdgcn_raw_buffer_load_b32(rs, WS_PSC + ((rt * 4 + kp) * 8 + wave) * 4, 0, 16));
        }
        u2 sv[2];
#pragma unroll
        for (int m = 0; m < 2; ++m) sv[m] = __builtin_amdgcn_raw_buffer_load_b64(rs, WS_S + ((2 * rt + m) * 28 + 4 * wave + ks) * 512 + lane * 8, 0, 16);
#pragma unroll
        for (int j = 0; j < 3; ++j) {
            own0[0] = fmaf(lo_h(pv[j][0]), psc[j], own0[0]); own0[1] = fmaf(hi_h(pv[j][0]), psc[j], own0[1]);
            own0[2] = fmaf(lo_h(pv[j][1]), psc[j], own0[2]); own0[3] = fmaf(hi_h(pv[j][1]), psc[j], own0[3]);
            own1[0] = fmaf(lo_h(pv[j][2]), psc[j], own1[0]); own1[1] = fmaf(hi_h(pv[j][2]), psc[j], own1[1]);
            own1[2] = fmaf(lo_h(pv[j][3]), psc[j], own1[2]); own1[3] = fmaf(hi_h(pv[j][3]), psc[j], own1[3]);
        }
#pragma unroll
        for (int m = 0; m < 2; ++m)
#pragma unroll
            for (int e = 0; e < 4; ++e) {
                float s = (e & 1) ? hi_f(sv[m][e >> 1]) : lo_f(sv[m][e >> 1]);
                float o = rbf(m ? own1[e] : own0[e]);
                float gg = rbf(o / (1.f + __expf(-o)));
                float r = bf2f(uu[m][e]) + rbf(s * gg);
                out[(size_t)(32 * rt + 16 * m + 4 * g + e) * D_ + cc] = (uint16_t)pk_bf16(r, 0.f);
            }
    }
    TR(8);
    TRFLUSH;
}

__device__ __forceinline__ f2 cmul(f2 a, f2 b) {
    f2 t = a * b.xx;
    f2 by = b.yy; by.x = -by.x;
    return __builtin_elementwise_fma(a.yx, by, t);
}
__device__ __forceinline__ f2 zpow(float a2, float wv, float p) {
    float e = __builtin_amdgcn_exp2f(a2 * p);
    float th = __builtin_amdgcn_fractf(wv * p);
    return (f2){e * __builtin_amdgcn_cosf(th), e * __builtin_amdgcn_sinf(th)};
}
__device__ __forceinline__ unsigned pkc(f2 v) { return pk_f16(v.x, v.y); }
__device__ __forceinline__ unsigned pkcn(f2 v) { return pk_f16(v.x, -v.y); }
#define KS_ROW 272
#define KS_WAVE (16 * KS_ROW)
#define SSM_KS   0
#define SSM_XC   0
#define SSM_KD   34816
#define SSM_BD   (34816 + 5248)
#define SSM_KR   40192
#define SSM_RED  0

__device__ __forceinline__ int kidx(int p) {
    unsigned bits = __float_as_uint((float)p) >> 16;
    return p < 128 ? p : (int)(bits - 0x4300u + 128u);
}

__device__ __forceinline__ void ssm_role(const uint16_t* __restrict__ lar, const uint16_t* __restrict__ aim,
                                         const uint16_t* __restrict__ cr, const uint16_t* __restrict__ ci,
                                         const uint16_t* __restrict__ ldt, const uint16_t* __restrict__ dsk,
                                         char* __restrict__ ws, char* lds, int sb) {
    const int tid = threadIdx.x, lane = tid & 63, wave = __builtin_amdgcn_readfirstlane(tid >> 6);
    const int r16 = lane & 15, g = lane >> 4;
    const int c0 = 2 * sb;
    const int ch = wave >> 2;
    TRDECL;
    TR(0);
    u64 tag = 0;
    const __amdgpu_buffer_rsrc_t rs = __builtin_amdgcn_make_buffer_rsrc(ws, 0, WS_BYTES, 0x00020000);
    {
        const int c = c0 + ch, n = 64 * (wave & 3) + lane;
        const float dt = rbf(expf(bf2f(ldt[c])));
        const float ar = -rbf(expf(bf2f(lar[c * N_ + n]))) * dt;
        const float ai = bf2f(aim[c * N_ + n]) * dt;
        float Cr = bf2f(cr[c * N_ + n]) * dt, Ci = bf2f(ci[c * N_ + n]) * dt;
        float bd = row16_sum(fabsf(Cr) + fabsf(Ci));
        bd += __shfl_xor(bd, 16); bd += __shfl_xor(bd, 32);
        TR(1);
        int eb = (int)((__float_as_uint(bd) >> 23) & 255u) - 126;
        eb = eb < -100 ? -100 : eb;
        const float S1 = __uint_as_float((unsigned)(127 + 14 - eb) << 23);
        const float S1inv = __uint_as_float((unsigned)(127 + eb - 27) << 23);
        const float a2 = ar * 1.44269504088896f, wv = ai * 0.159154943091895f;
        char* sc = lds + SSM_KS + wave * KS_WAVE;
        char* wp = sc + 4 * lane;
        const char* rp = sc + r16 * KS_ROW + 16 * g;
        f2 z1 = zpow(a2, wv, 1.f), z8 = zpow(a2, wv, 8.f), z64 = zpow(a2, wv, 64.f), z128 = zpow(a2, wv, 128.f), z1024 = zpow(a2, wv, 1024.f);
#define WR(e, v) *(unsigned*)(wp + (e) * KS_ROW) = (v)
        u4 bfr[4];
        {
            f2 cj = {16384.f, 0.f};
#pragma unroll
            for (int j = 0; j < 8; ++j) {
                f2 hi = cmul(cj, z64);
                WR(j, pkc(cj)); WR(8 + j, pkc(hi));
                if (j < 7) cj = cmul(cj, z8);
            }
#pragma unroll
            for (int k4 = 0; k4 < 4; ++k4) bfr[k4] = *(const u4*)(rp + 64 * k4);
        }
        f2 ze[8];
        ze[1] = z1; ze[2] = cmul(z1, z1); ze[3] = cmul(ze[2], z1); ze[4] = cmul(ze[2], ze[2]);
        ze[5] = cmul(ze[4], z1); ze[6] = cmul(ze[4], ze[2]); ze[7] = cmul(ze[4], ze[3]);
        f2 b = {Cr * S1, Ci * S1};
        f2 bh = cmul(b, z1024);
        f4 acc[3];
#pragma unroll
        for (int i = 0; i < 2; ++i) {
            WR(8 * i, pkcn(b));
#pragma unroll
            for (int e = 1; e < 8; ++e) WR(8 * i + e, pkcn(cmul(ze[e], b)));
            b = cmul(b, z128);
        }
        acc[0] = (f4){0, 0, 0, 0};
#pragma unroll
        for (int k4 = 0; k4 < 4; ++k4) acc[0] = mfma_h(*(const u4*)(rp + 64 * k4), bfr[k4], acc[0]);
#pragma unroll
        for (int i = 0; i < 2; ++i) {
            WR(4 * i, pkcn(b));
#pragma unroll
            for (int e = 1; e < 4; ++e) WR(4 * i + e, pkcn(cmul(ze[2 * e], b)));
            b = cmul(b, z128);
        }
#pragma unroll
        for (int i = 0; i < 4; ++i) {
            WR(8 + 2 * i, pkcn(b));
            WR(8 + 2 * i + 1, pkcn(cmul(b, ze[4])));
            if (i < 3) b = cmul(b, z128);
        }
        acc[1] = (f4){0, 0, 0, 0};
#pragma unroll
        for (int k4 = 0; k4 < 4; ++k4) acc[1] = mfma_h(*(const u4*)(rp + 64 * k4), bfr[k4], acc[1]);
#pragma unroll
        for (int i = 0; i < 9; ++i) {
            WR(i, pkcn(bh));
            if (i < 8) bh = cmul(bh, z128);
        }
        acc[2] = (f4){0, 0, 0, 0};
#pragma unroll
        for (int k4 = 0; k4 < 4; ++k4) acc[2] = mfma_h(*(const u4*)(rp + 64 * k4), bfr[k4], acc[2]);
#pragma unroll
        for (int T = 0; T < 3; ++T)
#pragma unroll
            for (int e = 0; e < 4; ++e) *(float*)(sc + ((16 * T + 4 * g + e) * 16 + r16) * 4) = acc[T][e] * S1inv;
        if (lane == 0) *(float*)(lds + SSM_BD + 4 * wave) = bd;
    }
    TR(2);
    if (wave == 0) {
        tag = __hip_atomic_load((gu64*)(ws + WS_EPOCH), RLX_AGENT) + 1ull;
        wait_flags((const gu64*)(ws + WS_FX) + ((c0 >> 4) / 7) * 64 + lane, true, tag);
    }
    __syncthreads();
    TR(3);
    u4 xq, xn;
    {
        const unsigned cb = WS_XT + (c0 >> 4) * 512 + (c0 & 15) * 8;
        xq = __builtin_amdgcn_raw_buffer_load_b128(rs, cb + (tid >> 2) * (28 * 512) + (tid & 3) * 128, 0, 16);
        const int t2 = tid + 1;
        xn = (u4){0, 0, 0, 0};
        if (tid < 511) xn = __builtin_amdgcn_raw_buffer_load_b128(rs, cb + (t2 >> 2) * (28 * 512) + (t2 & 3) * 128, 0, 16);
    }
    const int xch = tid >> 8, xa_i = tid & 255;
    for (int v = tid; v < 2 * 656; v += 512) {
        int cc = v >= 656, idx = v - 656 * cc;
        int row = idx >> 4, j = idx & 15;
        int p1 = row < 16 ? 128 * (row >> 3) + (row & 7) : row < 24 ? 256 + 128 * ((row - 16) >> 2) + 2 * (row & 3) : row < 32 ? 512 + 128 * ((row - 24) >> 1) + 4 * (row & 1) : 1024 + 128 * (row - 32);
        int p = p1 + 8 * j;
        const char* s0 = lds + SSM_KS + (4 * cc) * KS_WAVE + idx * 4;
        float s = (*(const float*)s0 + *(const float*)(s0 + KS_WAVE)) + (*(const float*)(s0 + 2 * KS_WAVE) + *(const float*)(s0 + 3 * KS_WAVE));
        if (p <= 2048) *(float*)(lds + SSM_KD + (656 * cc + kidx(p)) * 4) = s;
    }
    __syncthreads();
    TR(4);
    float SKinv0, SKinv1;
    {
        const float* bdp = (const float*)(lds + SSM_BD);
        float b0 = 2.f * ((bdp[0] + bdp[1]) + (bdp[2] + bdp[3])), b1 = 2.f * ((bdp[4] + bdp[5]) + (bdp[6] + bdp[7]));
        int e0 = (int)((__float_as_uint(b0) >> 23) & 255u) - 126, e1 = (int)((__float_as_uint(b1) >> 23) & 255u) - 126;
        e0 = e0 < -100 ? -100 : e0; e1 = e1 < -100 ? -100 : e1;
        SKinv0 = __uint_as_float((unsigned)(127 - 15 + e0) << 23); SKinv1 = __uint_as_float((unsigned)(127 - 15 + e1) << 23);
        const float SK = __uint_as_float((unsigned)(127 + 15 - (xch ? e1 : e0)) << 23);
        const float* kd = (const float*)(lds + SSM_KD) + 656 * xch;
        float v[8];
#pragma unroll
        for (int j = 0; j < 8; ++j) {
            int l = 2047 - (8 * xa_i + j);
            unsigned bits = pk_bf16((float)l, 0.f) & 0xffffu;
            int idx = l < 128 ? l : (int)(bits - 0x4300u + 128u);
            v[j] = kd[idx] * SK;
        }
        char* kr = lds + SSM_KR + xch * 4608;
        *(u4*)(kr + 16 * xa_i) = (u4){pk_f16(v[0], v[1]), pk_f16(v[2], v[3]), pk_f16(v[4], v[5]), pk_f16(v[6], v[7])};
        if (xa_i < 32) *(u4*)(kr + 4096 + 16 * xa_i) = (u4){0, 0, 0, 0};
    }
#pragma unroll
    for (int cc = 0; cc < 2; ++cc) {
        char* xc = lds + SSM_XC + cc * 16640 + 8 * (tid + 8);
        unsigned d0 = xq[2 * cc], d1 = xq[2 * cc + 1], d2 = xn[2 * cc], d3 = xn[2 * cc + 1];
        unsigned e0 = __builtin_amdgcn_alignbyte(d1, d0, 2), e1 = __builtin_amdgcn_alignbyte(d2, d1, 2), e2 = __builtin_amdgcn_alignbyte(d3, d2, 2);
        *(u2*)(xc) = (u2){d0, d1};
        *(u2*)(xc + 4160) = (u2){e0, e1};
        *(u2*)(xc + 2 * 4160) = (u2){d1, d2};
        *(u2*)(xc + 3 * 4160) = (u2){e1, e2};
        if (tid == 0) {
            char* x7 = lds + SSM_XC + cc * 16640 + 8 * 7;
            *(u2*)(x7) = (u2){0, 0};
            *(u2*)(x7 + 4160) = (u2){0, d0 << 16};
            *(u2*)(x7 + 2 * 4160) = (u2){0, d0};
            *(u2*)(x7 + 3 * 4160) = (u2){d0 << 16, e0};
        }
        if (tid >= 64 && tid < 64 + 28) { int z = tid - 64; *(u2*)(lds + SSM_XC + cc * 16640 + (z / 7) * 4160 + 8 * (z % 7)) = (u2){0, 0}; }
    }
    __syncthreads();
    TR(5);
    f4 acc[8];
#pragma unroll
    for (int n = 0; n < 8; ++n) acc[n] = (f4){0, 0, 0, 0};
    {
        const int rq = wave & 3;
        const char* xc = lds + SSM_XC + ch * 16640 + ((r16 + 1) & 3) * 4160 + 8 * (10 - 2 * g + ((r16 + 1) >> 2));
        const char* kr = lds + SSM_KR + ch * 4608 + 16 * (257 - 2 * r16 - g);
#pragma unroll
        for (int rr = 0; rr < 2; ++rr) {
            const int r = 2 * rq + rr;
            u4 af[8], bfv[8];
#pragma unroll
            for (int kp = 0; kp < 8; ++kp) {
                const char* p = xc + 64 * (8 * kp + r);
                u2 a = *(const u2*)p, b = *(const u2*)(p + 8);
                af[kp] = (u4){a.x, a.y, b.x, b.y};
            }
#pragma unroll
            for (int j = 0; j < 8; ++j) bfv[j] = *(const u4*)(kr - 64 * (8 * j - r));
#pragma unroll
            for (int n = 0; n < 8; ++n)
#pragma unroll
                for (int kp = 0; kp <= n; ++kp) acc[n] = mfma_h(af[kp], bfv[n - kp], acc[n]);
        }
    }
    const int t = 256 * wave + 16 * r16 + 4 * g;
    const u2 xx0 = *(const u2*)(lds + SSM_XC + 2 * (t + 32));
    const u2 xx1 = *(const u2*)(lds + SSM_XC + 16640 + 2 * (t + 32));
    TR(6);
    __syncthreads();
    {
        char* red = lds + SSM_RED;
#pragma unroll
        for (int n = 0; n < 8; ++n) *(f4*)(red + ((wave * 8 + n) * 64 + lane) * 16) = acc[n];
    }
    __syncthreads();
    TR(7);
    {
        const char* red = lds + SSM_RED;
        const int n = wave;
        f4 y0 = {0, 0, 0, 0}, y1 = {0, 0, 0, 0};
#pragma unroll
        for (int w = 0; w < 4; ++w) { y0 += *(const f4*)(red + ((w * 8 + n) * 64 + lane) * 16); y1 += *(const f4*)(red + (((w + 4) * 8 + n) * 64 + lane) * 16); }
        const float ds0 = bf2f(dsk[c0]), ds1 = bf2f(dsk[c0 + 1]);
        float s0[4], s1[4];
#pragma unroll
        for (int e = 0; e < 4; ++e) {
            float x0 = (e & 1) ? hi_h(xx0[e >> 1]) : lo_h(xx0[e >> 1]), x1 = (e & 1) ? hi_h(xx1[e >> 1]) : lo_h(xx1[e >> 1]);
            s0[e] = rbf(y0[e] * SKinv0) + rbf(x0 * ds0); s1[e] = rbf(y1[e] * SKinv1) + rbf(x1 * ds1);
        }
        const unsigned soff = WS_S + ((t >> 4) * 28 + (c0 >> 4)) * 512 + (g * 16 + (c0 & 15)) * 8;
        __builtin_amdgcn_raw_buffer_store_b128((u4){pk_bf16(s0[0], s0[1]), pk_bf16(s0[2], s0[3]), pk_bf16(s1[0], s1[1]), pk_bf16(s1[2], s1[3])}, rs, soff, 0, 16);
    }
    WAIT_VM(0);
    __syncthreads();
    if (tid == 0) __hip_atomic_store((gu64*)(ws + WS_FS) + sb, tag, RLX_AGENT);
    TR(8);
    TRFLUSH;
}

__global__ __launch_bounds__(512, 4) void fused_kernel(const uint16_t* __restrict__ u, const uint16_t* __restrict__ lar, const uint16_t* __restrict__ aim,
                                                       const uint16_t* __restrict__ cr, const uint16_t* __restrict__ ci,
                                                       const uint16_t* __restrict__ ldt, const uint16_t* __restrict__ dsk,
                                                       const uint16_t* __restrict__ w1, const uint16_t* __restrict__ w2,
                                                       char* __restrict__ ws, uint16_t* __restrict__ out) {
    extern __shared__ __attribute__((aligned(16))) char lds[];
    const int b = blockIdx.x;
    if (b < 256) ffn_role(u, w1, w2, ws, out, lds, b);
    else ssm_role(lar, aim, cr, ci, ldt, dsk, ws, lds, b - 256);
}

extern "C" void kernel_launch(void* const* d_in, const int* in_sizes, int n_in,
                              void* d_out, int out_size, void* d_ws, size_t ws_size,
                              hipStream_t stream) {
    (void)hipFuncSetAttribute((const void*)fused_kernel, hipFuncAttributeMaxDynamicSharedMemorySize, LDS_BYTES);
    fused_kernel<<<480, 512, LDS_BYTES, stream>>>((const uint16_t*)d_in[0], (const uint16_t*)d_in[1], (const uint16_t*)d_in[2], (const uint16_t*)d_in[3],
                                                  (const uint16_t*)d_in[4], (const uint16_t*)d_in[5], (const uint16_t*)d_in[6], (const uint16_t*)d_in[8],
                                                  (const uint16_t*)d_in[9], (char*)d_ws, (uint16_t*)d_out);
}
```

```cpp
#include <hip/hip_runtime.h>
#include <stdint.h>

#define L_ 2048
#define D_ 448
#define N_ 256
#define F_ 1792

typedef __attribute__((ext_vector_type(8))) __bf16 bf8;
typedef __attribute__((ext_vector_type(8))) _Float16 h8;
typedef __attribute__((ext_vector_type(4))) short s4v;
typedef __attribute__((ext_vector_type(4))) float f4;
typedef __attribute__((ext_vector_type(2))) float f2;
typedef __attribute__((ext_vector_type(4))) unsigned u4;
typedef __attribute__((ext_vector_type(2))) unsigned u2;
typedef __attribute__((ext_vector_type(2))) __bf16 bf2v;
typedef __attribute__((ext_vector_type(2))) _Float16 h2v;
typedef unsigned long long u64;
#define LAS __attribute__((address_space(3)))
#define GAS __attribute__((address_space(1)))
typedef GAS u64 gu64;
#define RLX_AGENT __ATOMIC_RELAXED, __HIP_MEMORY_SCOPE_AGENT

__device__ __forceinline__ float bf2f(uint16_t v) { return __uint_as_float(((uint32_t)v) << 16); }
__device__ __forceinline__ unsigned pk_bf16(float a, float b) { bf2v r; r.x = (__bf16)a; r.y = (__bf16)b; return __builtin_bit_cast(unsigned, r); }
__device__ __forceinline__ unsigned pk_f16(float a, float b) { h2v r; r.x = (_Float16)a; r.y = (_Float16)b; return __builtin_bit_cast(unsigned, r); }
__device__ __forceinline__ float rbf(float f) { return __uint_as_float(pk_bf16(f, 0.f) << 16); }
__device__ __forceinline__ float lo_f(unsigned w) { return __uint_as_float(w << 16); }
__device__ __forceinline__ float hi_f(unsigned w) { return __uint_as_float(w & 0xffff0000u); }
__device__ __forceinline__ float lo_h(unsigned w) { return (float)__builtin_bit_cast(h2v, w).x; }
__device__ __forceinline__ float hi_h(unsigned w) { return (float)__builtin_bit_cast(h2v, w).y; }
__device__ __forceinline__ f4 mfma_bf(u4 a, u4 b, f4 c) { return __builtin_amdgcn_mfma_f32_16x16x32_bf16(__builtin_bit_cast(bf8, a), __builtin_bit_cast(bf8, b), c, 0, 0, 0); }
__device__ __forceinline__ f4 mfma_h(u4 a, u4 b, f4 c) { return __builtin_amdgcn_mfma_f32_16x16x32_f16(__builtin_bit_cast(h8, a), __builtin_bit_cast(h8, b), c, 0, 0, 0); }
template <int CTRL> __device__ __forceinline__ float dpp_f(float v) { return __int_as_float(__builtin_amdgcn_update_dpp(0, __float_as_int(v), CTRL, 0xf, 0xf, false)); }
__device__ __forceinline__ float row16_sum(float v) { v += dpp_f<0x128>(v); v += dpp_f<0x124>(v); v += dpp_f<0x122>(v); v += dpp_f<0x121>(v); return v; }
__device__ __forceinline__ float row16_max(float v) { v = fmaxf(v, dpp_f<0x128>(v)); v = fmaxf(v, dpp_f<0x124>(v)); v = fmaxf(v, dpp_f<0x122>(v)); v = fmaxf(v, dpp_f<0x121>(v)); return v; }
__device__ __forceinline__ u2 tr1(const char* p) { return __builtin_bit_cast(u2, __builtin_amdgcn_ds_read_tr16_b64_v4i16((LAS s4v*)p)); }
__device__ __forceinline__ void dma_pair(unsigned dst, unsigned va, unsigned vb, const void* sbase) {
    asm volatile("s_mov_b32 m0, %0\n\ts_nop 0\n\tglobal_load_lds_dwordx4 %1, %3\n\tglobal_load_lds_dwordx4 %2, %3 offset:1024" :: "s"(dst), "v"(va), "v"(vb), "s"(sbase) : "memory");
}
#define WAIT_VM(n) asm volatile("s_waitcnt vmcnt(" #n ")" ::: "memory")
#define WAIT_LGKM0() asm volatile("s_waitcnt lgkmcnt(0)" ::: "memory")
#define RAW_BAR() do { WAIT_LGKM0(); __builtin_amdgcn_s_barrier(); asm volatile("" ::: "memory"); } while (0)

#define TRDECL
#define TR(i)
#define TRFLUSH
#define TRW 0
#define SLEEPN 4

#define XTM 4
#define ROTM 1
#define NFFN 128
#define NSSM 112
#define WS_EPOCH 0
#define NREP 16
#define WS_FX    65536
#define WS_ST    32768
#define WS_FS    131072
#define WS_FP    196608
#define WS_PSC   16384
#define WS_XT    (1u << 20)
#define WS_S     (3u << 20)
#define WS_PART  (5u << 20)
#define WS_BYTES (16u << 20)
#define LDS_BYTES 160000
#define SPIN_CAP (1u << 14)

__device__ __forceinline__ void wait_flags(const gu64* p, bool active, u64 tag) {
    for (unsigned spins = 0; spins < SPIN_CAP; ++spins) {
        u64 v = active ? __hip_atomic_load(p, RLX_AGENT) : tag;
        if (__all(v == tag)) break;
        __builtin_amdgcn_s_sleep(SLEEPN);
    }
    __builtin_amdgcn_fence(__ATOMIC_ACQUIRE, "wavefront");
}

#define XN_BYTES 57344
#define RING_OFF XN_BYTES
#define RSL 7
#define RING_W (RSL * 2048)
#define ST_OFF (RING_OFF + 7 * RING_W)
__device__ __forceinline__ void ffn_role(const uint16_t* __restrict__ u, const uint16_t* __restrict__ w1, const uint16_t* __restrict__ w2,
                                         char* ws, uint16_t* __restrict__ out, char* lds, int b) {
    const int tid = threadIdx.x, lane = tid & 63, wave = __builtin_amdgcn_readfirstlane(tid >> 6);
    const int r16 = lane & 15, g = lane >> 4;
    const int xcd = b & 7, ks = xcd & 3, rt = 16 * (xcd >> 2) + (b >> 3);
    const int rot = ROTM * ((b >> 3) % 14);
    TRDECL;
    TR(0);
    const __amdgpu_buffer_rsrc_t rs = __builtin_amdgcn_make_buffer_rsrc(ws, 0, WS_BYTES, 0x00020000);
    u4 ur[2][4];
    {
        const u4* up = (const u4*)(u + (size_t)(64 * rt + 8 * wave + g) * D_) + (r16 < 14 ? r16 : 13);
        const u4* up2 = up + 4 * 56;
        asm volatile("global_load_dwordx4 %0, %8, off\n\tglobal_load_dwordx4 %1, %8, off offset:224\n\tglobal_load_dwordx4 %2, %8, off offset:448\n\tglobal_load_dwordx4 %3, %8, off offset:672\n\t"
                     "global_load_dwordx4 %4, %9, off\n\tglobal_load_dwordx4 %5, %9, off offset:224\n\tglobal_load_dwordx4 %6, %9, off offset:448\n\tglobal_load_dwordx4 %7, %9, off offset:672"
                     : "=&v"(ur[0][0]), "=&v"(ur[0][1]), "=&v"(ur[0][2]), "=&v"(ur[0][3]), "=&v"(ur[1][0]), "=&v"(ur[1][1]), "=&v"(ur[1][2]), "=&v"(ur[1][3])
                     : "v"(up), "v"(up2) : "memory");
    }
    const unsigned ldsbase = (unsigned)(size_t)(LAS char*)lds;
    const unsigned ring = ldsbase + RING_OFF + wave * RING_W;
    const int rho = lane >> 3;
    const unsigned srcc = (unsigned)(((lane & 7) ^ (((rho >> 1) & 3) << 1)) * 16);
    const int krow = 8 * (rho >> 2) + (rho & 3);
    const unsigned v1a = krow * (F_ * 2) + srcc, v1b = v1a + 16 * F_ * 2 - 1024;
    const unsigned v2a = krow * (D_ * 2) + srcc, v2b = v2a + 16 * D_ * 2 - 1024;
    const char* w1b = (const char*)(w1 + 448 * ks + 64 * wave);
    const char* w2b = (const char*)(w2 + (size_t)448 * ks * D_ + 64 * wave);
#define KROT(i) ((i) + rot >= 14 ? (i) + rot - 14 : (i) + rot)
#define ISSUE(s) do { if ((s) < 28) dma_pair(ring + ((s) % RSL) * 2048, v1a, v1b, w1b + (size_t)KROT((s) >> 1) * (32 * F_ * 2) + ((s) & 1) * (4 * F_ * 2)); \
                      else if ((s) < 56) dma_pair(ring + ((s) % RSL) * 2048, v2a, v2b, w2b + (size_t)KROT(((s) - 28) >> 1) * (32 * D_ * 2) + ((s) & 1) * (4 * D_ * 2)); } while (0)
    u64 tag = 0;
    if (wave < 7) { ISSUE(0); ISSUE(1); ISSUE(2); ISSUE(3); ISSUE(4); ISSUE(5); ISSUE(6); }
    else WAIT_VM(0);
    TR(9);
    asm volatile("s_waitcnt vmcnt(14)" : "+v"(ur[0][0]), "+v"(ur[0][1]), "+v"(ur[0][2]), "+v"(ur[0][3]), "+v"(ur[1][0]), "+v"(ur[1][1]), "+v"(ur[1][2]), "+v"(ur[1][3]) :: "memory");
    TR(10);
    if (wave == 7) { __builtin_amdgcn_s_setprio(3); tag = *(const u64*)(ws + WS_EPOCH) + 1ull; }
#pragma unroll
    for (int p = 0; p < 2; ++p) {
        const bool act = r16 < 14;
        const bf2v one2 = {(__bf16)1.0f, (__bf16)1.0f};
        float s = 0.f;
#pragma unroll
        for (int i = 0; i < 4; ++i)
#pragma unroll
            for (int e = 0; e < 4; ++e) { const unsigned w_ = ur[p][i][e]; s = __builtin_amdgcn_fdot2_f32_bf16(__builtin_bit_cast(bf2v, w_), one2, s, false); }
        s = row16_sum(act ? s : 0.f);
        const float mean = s * (1.0f / D_);
        f2 xv[4][4];
        f2 q2 = {0.f, 0.f};
#pragma unroll
        for (int i = 0; i < 4; ++i)
#pragma unroll
            for (int e = 0; e < 4; ++e) {
                xv[i][e] = (f2){lo_f(ur[p][i][e]), hi_f(ur[p][i][e])};
                f2 d = xv[i][e] - (f2){mean, mean};
                q2 = __builtin_elementwise_fma(d, d, q2);
            }
        float q = row16_sum(act ? q2.x + q2.y : 0.f);
        const float mb = rbf(mean);
        const float sd = rbf(__builtin_amdgcn_sqrtf(rbf(rbf(q * (1.0f / D_)) + 1e-5f)));
        const float rinv = __builtin_amdgcn_rcpf(sd);
        const int lrow = 8 * wave + 4 * p + g;
        if (r16 == 0) *(unsigned*)(lds + ST_OFF + 4 * lrow) = (__float_as_uint(mb) >> 16) | (__float_as_uint(sd) & 0xffff0000u);
        char* xr = lds + lrow * 896;
        const int sw = (lrow >> 1) & 7;
#pragma unroll
        for (int i = 0; i < 4; ++i) {
            u4 o;
#pragma unroll
            for (int e = 0; e < 4; ++e) {
                f2 d = xv[i][e] - (f2){mb, mb};
                unsigned a = pk_bf16(d.x, d.y);
                f2 r = (f2){lo_f(a), hi_f(a)} * (f2){rinv, rinv};
                o[e] = pk_bf16(r.x, r.y);
            }
            if (act) *(u4*)(xr + 16 * ((r16 + 14 * i) ^ sw)) = o;
        }
    }
    if (tid == 0) *(unsigned*)(lds + ST_OFF + 256) = 0u;
    TR(1);
    RAW_BAR();
    TR(2);
    const int q4 = r16 >> 2, p4 = lane & 3;
    if (wave == 7) {
        if (ks == 0) {
            __builtin_amdgcn_raw_buffer_store_b32(*(const unsigned*)(lds + ST_OFF + 4 * lane), rs, WS_ST + (64 * rt + lane) * 4, 0, 16);
            WAIT_VM(0);
            if (lane < NREP) __hip_atomic_store((gu64*)(ws + WS_FX) + lane * 64 + rt, tag, RLX_AGENT);
        }
        {
            const u4 ones = {0xffffffffu, 0xffffffffu, 0xffffffffu, 0xffffffffu};
            const unsigned pb = WS_PART + (unsigned)((rt * 4 + ks) * 56) * 1024 + lane * 16;
            for (int i = 0; i < 56; ++i) if (((i >> 1) & 3) != ks) __builtin_amdgcn_raw_buffer_store_b128(ones, rs, pb + i * 1024, 0, 16);
            if (lane < 8) __builtin_amdgcn_raw_buffer_store_b32(0xffffffffu, rs, WS_PSC + ((rt * 4 + ks) * 8 + lane) * 4, 0, 16);
            WAIT_VM(0);
            if (lane == 0) __hip_atomic_store((gu64*)(ws + WS_FP) + rt * 16 + ks, tag, RLX_AGENT);
        }
        __builtin_amdgcn_s_setprio(0);
        RAW_BAR();
        RAW_BAR();
        wait_flags((const gu64*)(ws + WS_FS) + (rt & (NREP - 1)) * 128 + lane, true, tag);
        {
            const gu64* p = lane < 48 ? (const gu64*)(ws + WS_FS) + (rt & (NREP - 1)) * 128 + 64 + lane : (const gu64*)(ws + WS_FP) + rt * 16 + (lane & 3);
            wait_flags(p, lane < 52, tag);
        }
        if (lane == 0) __hip_atomic_store((LAS unsigned*)((LAS char*)lds + ST_OFF + 256), 1u, __ATOMIC_RELAXED, __HIP_MEMORY_SCOPE_WORKGROUP);
        if (b == 0) {
#pragma unroll
            for (int i = 0; i < 2; ++i) wait_flags((const gu64*)(ws + WS_FP) + (16 * i + (lane >> 2)) * 16 + (lane & 3), true, tag);
            if (lane == 0) __hip_atomic_store((gu64*)(ws + WS_EPOCH), tag, RLX_AGENT);
        }
        return;
    }
    const int rr = 4 * g + q4;
    const int wtr = rr * 128 + (((rr >> 1) & 3) << 5) + 8 * p4;
    const char* rpb = lds + RING_OFF + wave * RING_W;
    const char* rp0 = rpb + wtr; const char* rp1 = rpb + (wtr ^ 32); const char* rp2 = rpb + (wtr ^ 64); const char* rp3 = rpb + (wtr ^ 96);
    const int asw = (r16 >> 1) & 7;
    const unsigned ae = (unsigned)((g ^ asw) * 16);
    const char* a0p = lds + r16 * 896;
    f4 acc[4][4];
#pragma unroll
    for (int m = 0; m < 4; ++m)
#pragma unroll
        for (int n = 0; n < 4; ++n) acc[m][n] = (f4){0, 0, 0, 0};
    u4 af[2][4]; u2 x[2][2][4];
#define VMW(n) do { if ((n) >= 10) WAIT_VM(10); else if ((n) >= 8) WAIT_VM(8); else if ((n) >= 6) WAIT_VM(6); else if ((n) >= 4) WAIT_VM(4); else if ((n) >= 2) WAIT_VM(2); else WAIT_VM(0); } while (0)
#define LOADW(j, bsel, issued) do { VMW(2 * ((issued) - (2 * (j) + 1))); \
        _Pragma("unroll") for (int hf = 0; hf < 2; ++hf) { const int so = ((2 * (j) + hf) % RSL) * 2048; \
            x[bsel][hf][0] = tr1(rp0 + so); x[bsel][hf][1] = tr1(rp1 + so); x[bsel][hf][2] = tr1(rp2 + so); x[bsel][hf][3] = tr1(rp3 + so); } } while (0)
#define LOADA(it, bsel) do { const char* ap = a0p + (ae ^ (unsigned)(KROT(it) << 6)); \
        _Pragma("unroll") for (int m = 0; m < 4; ++m) af[bsel][m] = *(const u4*)(ap + m * 16 * 896); } while (0)
#define REFILL(j) do { WAIT_LGKM0(); ISSUE(2 * (j) + RSL); ISSUE(2 * (j) + 1 + RSL); } while (0)
#define ISSUED(j) ((2 * (j) + 1 + RSL) < 55 ? (2 * (j) + 1 + RSL) : 55)
    LOADW(0, 0, RSL - 1); LOADA(0, 0);
    REFILL(0);
#pragma unroll
    for (int it = 0; it < 14; ++it) {
        const int cur = it & 1, nxt = cur ^ 1;
        LOADW(it + 1, nxt, ISSUED(it));
        if (it < 13) LOADA(it + 1, nxt);
        __builtin_amdgcn_sched_barrier(0);
#pragma unroll
        for (int n = 0; n < 4; ++n) {
            const u4 wf = {x[cur][0][n].x, x[cur][0][n].y, x[cur][1][n].x, x[cur][1][n].y};
#pragma unroll
            for (int m = 0; m < 4; ++m) acc[m][n] = mfma_bf(wf, af[cur][m], acc[m][n]);
            __builtin_amdgcn_sched_barrier(0);
            if (n == 1) { WAIT_LGKM0(); ISSUE(2 * (it + 1) + RSL); }
            if (n == 2) { ISSUE(2 * (it + 1) + 1 + RSL); }
            __builtin_amdgcn_sched_barrier(0);
        }
    }
    TR(3);
    RAW_BAR();
#pragma unroll
    for (int m = 0; m < 4; ++m)
#pragma unroll
        for (int n = 0; n < 4; ++n) {
            u2 o = {pk_bf16(fmaxf(acc[m][n][0], 0.f), fmaxf(acc[m][n][1], 0.f)), pk_bf16(fmaxf(acc[m][n][2], 0.f), fmaxf(acc[m][n][3], 0.f))};
            *(u2*)(lds + (16 * m + r16) * 896 + 16 * ((8 * wave + 2 * n + (g >> 1)) ^ asw) + 8 * (g & 1)) = o;
            acc[m][n] = (f4){0, 0, 0, 0};
        }
    RAW_BAR();
    TR(4);
    LOADA(0, 0);
#pragma unroll
    for (int it = 0; it < 14; ++it) {
        const int cur = it & 1, nxt = cur ^ 1;
        if (it < 13) { LOADW(14 + it + 1, nxt, ISSUED(14 + it)); LOADA(it + 1, nxt); }
        __builtin_amdgcn_sched_barrier(0);
#pragma unroll
        for (int n = 0; n < 4; ++n) {
            const u4 wf = {x[cur][0][n].x, x[cur][0][n].y, x[cur][1][n].x, x[cur][1][n].y};
#pragma unroll
            for (int m = 0; m < 4; ++m) acc[m][n] = mfma_bf(wf, af[cur][m], acc[m][n]);
            __builtin_amdgcn_sched_barrier(0);
            if (n == 1 && 2 * (14 + it + 1) + RSL < 56) { WAIT_LGKM0(); ISSUE(2 * (14 + it + 1) + RSL); }
            if (n == 2 && 2 * (14 + it + 1) + RSL < 56) { ISSUE(2 * (14 + it + 1) + 1 + RSL); }
            __builtin_amdgcn_sched_barrier(0);
        }
    }
    TR(5);
    {
        float mx = 0.f;
#pragma unroll
        for (int m = 0; m < 4; ++m)
#pragma unroll
            for (int n = 0; n < 4; ++n)
#pragma unroll
                for (int e = 0; e < 4; ++e) mx = fmaxf(mx, fabsf(acc[m][n][e]));
        mx = row16_max(mx);
        mx = fmaxf(mx, __shfl_xor(mx, 16)); mx = fmaxf(mx, __shfl_xor(mx, 32));
        int e2 = (int)((__float_as_uint(mx) >> 23) & 255u) - 126;
        e2 = e2 < -100 ? -100 : e2;
        const float sc = __uint_as_float((unsigned)(127 + 15 - e2) << 23);
        const float scinv = __uint_as_float((unsigned)(127 - 15 + e2) << 23);
        const unsigned pbase = WS_PART + (unsigned)(((rt * 4 + ks) * 7 + wave) * 8) * 1024 + lane * 16;
#pragma unroll
        for (int m = 0; m < 4; ++m) {
            if (m != ks) {
#pragma unroll
                for (int np = 0; np < 2; ++np) {
                    u4 o = {pk_f16(acc[m][2 * np][0] * sc, acc[m][2 * np][1] * sc), pk_f16(acc[m][2 * np][2] * sc, acc[m][2 * np][3] * sc),
                            pk_f16(acc[m][2 * np + 1][0] * sc, acc[m][2 * np + 1][1] * sc), pk_f16(acc[m][2 * np + 1][2] * sc, acc[m][2 * np + 1][3] * sc)};
#pragma unroll
                    for (int e = 0; e < 4; ++e) o[e] = o[e] < 0xfffffffeu ? o[e] : 0xfffffffeu;
                    __builtin_amdgcn_raw_buffer_store_b128(o, rs, pbase + (2 * m + np) * 1024, 0, 16);
                }
            }
        }
        if (lane == 0) __builtin_amdgcn_raw_buffer_store_b32(__float_as_uint(scinv), rs, WS_PSC + ((rt * 4 + ks) * 8 + wave) * 4, 0, 16);
    }
    f4 own[4];
#pragma unroll
    for (int n = 0; n < 4; ++n) own[n] = ks == 0 ? acc[0][n] : ks == 1 ? acc[1][n] : ks == 2 ? acc[2][n] : acc[3][n];
    TR(6);
    const int trow = 64 * rt + 16 * ks;
    const size_t uo = (size_t)(trow + (lane >> 3)) * D_ + 64 * wave + 8 * (lane & 7);
    const u4 uu0 = *(const u4*)(u + uo), uu1 = *(const u4*)(u + uo + 8 * D_);
    for (unsigned spins = 0; spins < (1u << 20); ++spins) {
        if (__hip_atomic_load((LAS unsigned*)((LAS char*)lds + ST_OFF + 256), __ATOMIC_RELAXED, __HIP_MEMORY_SCOPE_WORKGROUP)) break;
        __builtin_amdgcn_s_sleep(2);
    }
    asm volatile("" ::: "memory");
    TR(7);
    {
        u2 sv[4];
#pragma unroll
        for (int n = 0; n < 4; ++n) sv[n] = __builtin_amdgcn_raw_buffer_load_b64(rs, WS_S + (16 * wave + 4 * n + g) * 16384 + (trow + r16) * 8, 0, 16);
        u4 pv[3][2]; float psc[3];
        for (unsigned spins = 0; spins < SPIN_CAP; ++spins) {
            unsigned mxv = 0;
#pragma unroll
            for (int j = 0; j < 3; ++j) {
                const int kp = (ks + 1 + j) & 3;
#pragma unroll
                for (int np = 0; np < 2; ++np)
                    pv[j][np] = __builtin_amdgcn_raw_buffer_load_b128(rs, WS_PART + (unsigned)((((rt * 4 + kp) * 7 + wave) * 8) + 2 * ks + np) * 1024 + lane * 16, 0, 16);
                psc[j] = __uint_as_float(__builtin_amdgcn_raw_buffer_load_b32(rs, WS_PSC + ((rt * 4 + kp) * 8 + wave) * 4, 0, 16));
            }
#pragma unroll
            for (int j = 0; j < 3; ++j) {
#pragma unroll
                for (int np = 0; np < 2; ++np)
#pragma unroll
                    for (int e = 0; e < 4; ++e) { const unsigned w_ = pv[j][np][e]; mxv = mxv > w_ ? mxv : w_; }
                const unsigned w_ = __float_as_uint(psc[j]); mxv = mxv > w_ ? mxv : w_;
            }
            if (!__any(mxv == 0xffffffffu)) break;
            __builtin_amdgcn_s_sleep(1);
            asm volatile("" ::: "memory");
        }
        TR(11);
#pragma unroll
        for (int j = 0; j < 3; ++j)
#pragma unroll
            for (int n = 0; n < 4; ++n) {
                const unsigned w0 = pv[j][n >> 1][2 * (n & 1)], w1_ = pv[j][n >> 1][2 * (n & 1) + 1];
                own[n][0] = fmaf(lo_h(w0), psc[j], own[n][0]); own[n][1] = fmaf(hi_h(w0), psc[j], own[n][1]);
                own[n][2] = fmaf(lo_h(w1_), psc[j], own[n][2]); own[n][3] = fmaf(hi_h(w1_), psc[j], own[n][3]);
            }
        char* tb = lds + RING_OFF + wave * RING_W;
#pragma unroll
        for (int n = 0; n < 4; ++n) {
            float sg[4];
#pragma unroll
            for (int e = 0; e < 4; ++e) {
                const unsigned sw_ = sv[n][e >> 1];
                float s = (e & 1) ? hi_f(sw_) : lo_f(sw_);
                float o = rbf(own[n][e]);
                float gg = rbf(o * __builtin_amdgcn_rcpf(1.f + __expf(-o)));
                sg[e] = s * gg;
            }
            *(u2*)(tb + r16 * 144 + 32 * n + 8 * g) = (u2){pk_bf16(sg[0], sg[1]), pk_bf16(sg[2], sg[3])};
        }
#pragma unroll
        for (int h = 0; h < 2; ++h) {
            const u4 sgv = *(const u4*)(tb + (8 * h + (lane >> 3)) * 144 + 16 * (lane & 7));
            const u4 uv = h ? uu1 : uu0;
            u4 o;
#pragma unroll
            for (int e = 0; e < 4; ++e) { const unsigned a = uv[e], c = sgv[e]; o[e] = pk_bf16(lo_f(a) + lo_f(c), hi_f(a) + hi_f(c)); }
            *(u4*)(out + uo + (size_t)h * 8 * D_) = o;
        }
    }
    TR(8);
    TRFLUSH;
}

__device__ __forceinline__ f2 cmul(f2 a, f2 b) {
    f2 t = a * b.xx;
    f2 by = b.yy; by.x = -by.x;
    return __builtin_elementwise_fma(a.yx, by, t);
}
__device__ __forceinline__ f2 zpow(float a2, float wv, float p) {
    float e = __builtin_amdgcn_exp2f(a2 * p);
    float th = __builtin_amdgcn_fractf(wv * p);
    return (f2){e * __builtin_amdgcn_cosf(th), e * __builtin_amdgcn_sinf(th)};
}
struct c2 { f2 a, b; };
__device__ __forceinline__ c2 cmul2(c2 x, c2 y) { c2 o; o.a = cmul(x.a, y.a); o.b = cmul(x.b, y.b); return o; }
__device__ __forceinline__ u2 pkc(c2 v) { return (u2){pk_f16(v.a.x, v.a.y), pk_f16(v.b.x, v.b.y)}; }
__device__ __forceinline__ u2 pkcn(c2 v) { return (u2){pk_f16(v.a.x, -v.a.y), pk_f16(v.b.x, -v.b.y)}; }
#define KS_ROW 528
#define KS_WAVE (16 * KS_ROW)
#define SSM_KS   0
#define SSM_XC   67584
#define SSM_KR   134144
#define SSM_KD   152576
#define SSM_BD   157824
#define SSM_RED  0

__device__ __forceinline__ void ssm_role(const uint16_t* __restrict__ u, const uint16_t* __restrict__ lar, const uint16_t* __restrict__ aim,
                                         const uint16_t* __restrict__ cr, const uint16_t* __restrict__ ci,
                                         const uint16_t* __restrict__ ldt, const uint16_t* __restrict__ dsk,
                                         char* ws, char* lds, int sb) {
    const int tid = threadIdx.x, lane = tid & 63, wave = __builtin_amdgcn_readfirstlane(tid >> 6);
    const int r16 = lane & 15, g = lane >> 4;
    const int c0 = 4 * sb;
    const int ch = wave >> 1;
    TRDECL;
    TR(0);
    u64 tag = 0;
    const __amdgpu_buffer_rsrc_t rs = __builtin_amdgcn_make_buffer_rsrc(ws, 0, WS_BYTES, 0x00020000);
    u2 ucol[4];
    {
        const int c = c0 + ch, n0 = c * N_ + 128 * (wave & 1) + 2 * lane;
        const unsigned wl = *(const unsigned*)(lar + n0), wa = *(const unsigned*)(aim + n0), wr = *(const unsigned*)(cr + n0), wi = *(const unsigned*)(ci + n0);
        const uint16_t wdt = ldt[c];
        if (wave == 7) tag = *(const u64*)(ws + WS_EPOCH) + 1ull;
        const float dt = rbf(expf(bf2f(wdt)));
        const float ar0 = -rbf(expf(lo_f(wl))) * dt, ar1 = -rbf(expf(hi_f(wl))) * dt;
        const float ai0 = lo_f(wa) * dt, ai1 = hi_f(wa) * dt;
        const float Cr0 = lo_f(wr) * dt, Cr1 = hi_f(wr) * dt, Ci0 = lo_f(wi) * dt, Ci1 = hi_f(wi) * dt;
        float bd = row16_sum((fabsf(Cr0) + fabsf(Ci0)) + (fabsf(Cr1) + fabsf(Ci1)));
        bd += __shfl_xor(bd, 16); bd += __shfl_xor(bd, 32);
        TR(1);
        int eb = (int)((__float_as_uint(bd) >> 23) & 255u) - 126;
        eb = eb < -100 ? -100 : eb;
        const float S1 = __uint_as_float((unsigned)(127 + 14 - eb) << 23);
        const float S1inv = __uint_as_float((unsigned)(127 + eb - 27) << 23);
        const float a20 = ar0 * 1.44269504088896f, wv0 = ai0 * 0.159154943091895f, a21 = ar1 * 1.44269504088896f, wv1 = ai1 * 0.159154943091895f;
        char* sc = lds + SSM_KS + wave * KS_WAVE;
        char* wp = sc + 8 * lane;
        const char* rp = sc + r16 * KS_ROW + 16 * g;
#define ZP(p) (c2){zpow(a20, wv0, p), zpow(a21, wv1, p)}
        c2 z1 = ZP(1.f), z8 = ZP(8.f), z64 = ZP(64.f), z128 = ZP(128.f), z1024 = ZP(1024.f);
#define WR(e, v) *(u2*)(wp + (e) * KS_ROW) = (v)
        u4 bfr[8];
        {
            c2 cj = {(f2){16384.f, 0.f}, (f2){16384.f, 0.f}};
#pragma unroll
            for (int j = 0; j < 8; ++j) {
                c2 hi = cmul2(cj, z64);
                WR(j, pkc(cj)); WR(8 + j, pkc(hi));
                if (j < 7) cj = cmul2(cj, z8);
            }
#pragma unroll
            for (int k4 = 0; k4 < 8; ++k4) bfr[k4] = *(const u4*)(rp + 64 * k4);
        }
        asm volatile("" :: "v"(bfr[0]) : "memory");
#pragma unroll
        for (int e = 0; e < 4; ++e) ucol[e] = *(const u2*)(u + (size_t)(4 * tid + e) * D_ + c0);
        c2 ze[8];
        ze[1] = z1; ze[2] = cmul2(z1, z1); ze[3] = cmul2(ze[2], z1); ze[4] = cmul2(ze[2], ze[2]);
        ze[5] = cmul2(ze[4], z1); ze[6] = cmul2(ze[4], ze[2]); ze[7] = cmul2(ze[4], ze[3]);
        c2 b = {(f2){Cr0 * S1, Ci0 * S1}, (f2){Cr1 * S1, Ci1 * S1}};
        c2 bh = cmul2(b, z1024);
        f4 acc[3];
#pragma unroll
        for (int i = 0; i < 2; ++i) {
            WR(8 * i, pkcn(b));
#pragma unroll
            for (int e = 1; e < 8; ++e) WR(8 * i + e, pkcn(cmul2(ze[e], b)));
            b = cmul2(b, z128);
        }
        acc[0] = (f4){0, 0, 0, 0};
#pragma unroll
        for (int k4 = 0; k4 < 8; ++k4) acc[0] = mfma_h(*(const u4*)(rp + 64 * k4), bfr[k4], acc[0]);
#pragma unroll
        for (int i = 0; i < 2; ++i) {
            WR(4 * i, pkcn(b));
#pragma unroll
            for (int e = 1; e < 4; ++e) WR(4 * i + e, pkcn(cmul2(ze[2 * e], b)));
            b = cmul2(b, z128);
        }
#pragma unroll
        for (int i = 0; i < 4; ++i) {
            WR(8 + 2 * i, pkcn(b));
            WR(8 + 2 * i + 1, pkcn(cmul2(b, ze[4])));
            if (i < 3) b = cmul2(b, z128);
        }
        acc[1] = (f4){0, 0, 0, 0};
#pragma unroll
        for (int k4 = 0; k4 < 8; ++k4) acc[1] = mfma_h(*(const u4*)(rp + 64 * k4), bfr[k4], acc[1]);
#pragma unroll
        for (int i = 0; i < 9; ++i) {
            WR(i, pkcn(bh));
            if (i < 8) bh = cmul2(bh, z128);
        }
        acc[2] = (f4){0, 0, 0, 0};
#pragma unroll
        for (int k4 = 0; k4 < 8; ++k4) acc[2] = mfma_h(*(const u4*)(rp + 64 * k4), bfr[k4], acc[2]);
        float* pd = (float*)sc;
        *(f4*)(pd + 128 * (g >> 1) + 4 * (g & 1) + 8 * r16) = acc[0] * S1inv;
        if (g < 2) *(f4*)(pd + 256 + 64 * g + 4 * r16) = acc[1] * S1inv;
        else {
            *(f2*)(pd + 384 + 64 * (g - 2) + 2 * r16) = (f2){acc[1][0], acc[1][1]} * S1inv;
            *(f2*)(pd + 384 + 64 * (g - 2) + 32 + 2 * r16) = (f2){acc[1][2], acc[1][3]} * S1inv;
        }
        if (g < 3) {
#pragma unroll
            for (int e = 0; e < 4; ++e) pd[512 + 16 * (4 * g + e) + r16] = acc[2][e] * S1inv;
        }
        if (lane == 0) *(float*)(lds + SSM_BD + 4 * wave) = bd;
    }
    TR(2);
    __syncthreads();
    float SKinv[4];
    {
        const float* bdp = (const float*)(lds + SSM_BD);
        float SKs[4];
#pragma unroll
        for (int c = 0; c < 4; ++c) {
            float bb = 2.f * (bdp[2 * c] + bdp[2 * c + 1]);
            int e0 = (int)((__float_as_uint(bb) >> 23) & 255u) - 126; e0 = e0 < -100 ? -100 : e0;
            SKinv[c] = __uint_as_float((unsigned)(127 - 15 + e0) << 23); SKs[c] = __uint_as_float((unsigned)(127 + 15 - e0) << 23);
        }
        const int c = tid >> 7, q = tid & 127;
        const float SK = c == 0 ? SKs[0] : c == 1 ? SKs[1] : c == 2 ? SKs[2] : SKs[3];
#pragma unroll
        for (int rep = 0; rep < 2; ++rep) {
            const int qq = q + 128 * rep;
            if (qq < 164) {
                const char* s0 = lds + SSM_KS + (2 * c) * KS_WAVE + qq * 16;
                f4 v = (*(const f4*)s0 + *(const f4*)(s0 + KS_WAVE)) * SK;
                *(u2*)(lds + SSM_KD + c * 1312 + qq * 8) = (u2){pk_f16(v[0], v[1]), pk_f16(v[2], v[3])};
            }
        }
    }
    __syncthreads();
    TR(3);
    {
        const int xa_i = tid & 255;
        int idx[8];
#pragma unroll
        for (int j = 0; j < 8; j += 2) {
            int l0 = 2047 - (8 * xa_i + j), l1 = l0 - 1;
            unsigned bits = pk_bf16((float)l0, (float)l1);
            idx[j] = l0 < 128 ? l0 : (int)((bits & 0xffffu) - 0x4280u);
            idx[j + 1] = l1 < 128 ? l1 : (int)((bits >> 16) - 0x4280u);
        }
#pragma unroll
        for (int rep = 0; rep < 2; ++rep) {
            const int c = (tid >> 8) + 2 * rep;
            const uint16_t* kd = (const uint16_t*)(lds + SSM_KD + c * 1312);
            u4 o;
#pragma unroll
            for (int j = 0; j < 4; ++j) o[j] = (unsigned)kd[idx[2 * j]] | ((unsigned)kd[idx[2 * j + 1]] << 16);
            char* kr = lds + SSM_KR + c * 4608;
            *(u4*)(kr + 16 * xa_i) = o;
            if (xa_i < 32) *(u4*)(kr + 4096 + 16 * xa_i) = (u4){0, 0, 0, 0};
        }
        if (tid < 128) { *(u2*)(lds + SSM_XC + (tid >> 5) * 16640 + ((tid >> 3) & 3) * 4160 + 8 * (tid & 7)) = (u2){0, 0}; }
    }
    if (wave == 7) wait_flags((const gu64*)(ws + WS_FX) + (sb & (NREP - 1)) * 64 + (lane & 31), true, tag);
    __syncthreads();
    TR(4);
    {
        const u4 st = __builtin_amdgcn_raw_buffer_load_b128(rs, WS_ST + tid * 16, 0, 16);
        float xv[4][4];
#pragma unroll
        for (int e = 0; e < 4; ++e) {
            const unsigned sw_ = st[e];
            const float mb = lo_f(sw_), rinv = __builtin_amdgcn_rcpf(hi_f(sw_));
#pragma unroll
            for (int h = 0; h < 2; ++h) {
                const unsigned uw = ucol[e][h];
                unsigned a = pk_bf16(lo_f(uw) - mb, hi_f(uw) - mb);
                unsigned r = pk_bf16(lo_f(a) * rinv, hi_f(a) * rinv);
                xv[2 * h][e] = lo_f(r); xv[2 * h + 1][e] = hi_f(r);
            }
        }
#pragma unroll
        for (int cc = 0; cc < 4; ++cc) {
            const unsigned d0 = pk_f16(xv[cc][0], xv[cc][1]), d1 = pk_f16(xv[cc][2], xv[cc][3]);
            const unsigned n0 = __shfl_down(d0, 1), n1 = __shfl_down(d1, 1);
            const unsigned mid = __builtin_amdgcn_alignbyte(d1, d0, 2), e1 = __builtin_amdgcn_alignbyte(n0, d1, 2), e2 = __builtin_amdgcn_alignbyte(n1, n0, 2);
            char* xc = lds + SSM_XC + cc * 16640 + 8 * (tid + 8);
            *(u2*)(xc) = (u2){d0, d1};
            if (lane < 63) {
                *(u2*)(xc + 4160) = (u2){mid, e1};
                *(u2*)(xc + 2 * 4160) = (u2){d1, n0};
                *(u2*)(xc + 3 * 4160) = (u2){e1, e2};
            } else {
                *(unsigned*)(xc + 4160) = mid; *(uint16_t*)(xc + 4160 + 4) = (uint16_t)(d1 >> 16);
                *(unsigned*)(xc + 2 * 4160) = d1;
                *(uint16_t*)(xc + 3 * 4160) = (uint16_t)(d1 >> 16);
            }
            if (lane == 0) {
                *(uint16_t*)(xc + 4160 - 2) = (uint16_t)d0;
                *(unsigned*)(xc + 2 * 4160 - 4) = d0;
                *(uint16_t*)(xc + 3 * 4160 - 6) = (uint16_t)d0; *(unsigned*)(xc + 3 * 4160 - 4) = mid;
            }
        }
    }
    __syncthreads();
    TR(5);
    f4 acc[8];
#pragma unroll
    for (int n = 0; n < 8; ++n) acc[n] = (f4){0, 0, 0, 0};
    {
        const int rh = wave & 1;
        const char* xc = lds + SSM_XC + ch * 16640 + ((r16 + 1) & 3) * 4160 + 8 * (10 - 2 * g + ((r16 + 1) >> 2)) + 64 * 4 * rh;
        const char* kr = lds + SSM_KR + ch * 4608 + 16 * (257 - 2 * r16 - g) + 64 * 4 * rh;
#pragma unroll
        for (int rr = 0; rr < 4; ++rr) {
            u4 af[8], bfv[8];
#pragma unroll
            for (int kp = 0; kp < 8; ++kp) {
                const char* p = xc + 64 * (8 * kp + rr);
                u2 a = *(const u2*)p, b = *(const u2*)(p + 8);
                af[kp] = (u4){a.x, a.y, b.x, b.y};
            }
#pragma unroll
            for (int j = 0; j < 8; ++j) bfv[j] = *(const u4*)(kr - 64 * (8 * j - rr));
#pragma unroll
            for (int n = 0; n < 8; ++n)
#pragma unroll
                for (int kp = 0; kp <= n; ++kp) acc[n] = mfma_h(af[kp], bfv[n - kp], acc[n]);
        }
    }
    TR(6);
    {
        char* red = lds + SSM_RED;
#pragma unroll
        for (int n = 0; n < 8; ++n) *(f4*)(red + ((wave * 8 + n) * 64 + lane) * 16) = acc[n];
    }
    __syncthreads();
    TR(7);
    {
        const char* red = lds + SSM_RED;
        const int t = 256 * wave + 16 * r16 + 4 * g;
        float s[4][4];
#pragma unroll
        for (int c = 0; c < 4; ++c) {
            f4 y = *(const f4*)(red + (((2 * c) * 8 + wave) * 64 + lane) * 16) + *(const f4*)(red + (((2 * c + 1) * 8 + wave) * 64 + lane) * 16);
            const u2 xx = *(const u2*)(lds + SSM_XC + c * 16640 + 2 * (t + 32));
            const float ds = bf2f(dsk[c0 + c]);
#pragma unroll
            for (int e = 0; e < 4; ++e) {
                const unsigned xw = xx[e >> 1];
                float x = (e & 1) ? hi_h(xw) : lo_h(xw);
                s[c][e] = rbf(y[e] * SKinv[c]) + rbf(x * ds);
            }
        }
        const unsigned soff = WS_S + sb * 16384 + t * 8;
        __builtin_amdgcn_raw_buffer_store_b128((u4){pk_bf16(s[0][0], s[1][0]), pk_bf16(s[2][0], s[3][0]), pk_bf16(s[0][1], s[1][1]), pk_bf16(s[2][1], s[3][1])}, rs, soff, 0, 16);
        __builtin_amdgcn_raw_buffer_store_b128((u4){pk_bf16(s[0][2], s[1][2]), pk_bf16(s[2][2], s[3][2]), pk_bf16(s[0][3], s[1][3]), pk_bf16(s[2][3], s[3][3])}, rs, soff + 16, 0, 16);
    }
    WAIT_VM(0);
    __syncthreads();
    if (tid >= 448 && tid < 448 + NREP) __hip_atomic_store((gu64*)(ws + WS_FS) + (tid - 448) * 128 + sb, tag, RLX_AGENT);
    TR(8);
    TRFLUSH;
}

__global__ __launch_bounds__(512, 2) void fused_kernel(const uint16_t* __restrict__ u, const uint16_t* __restrict__ lar, const uint16_t* __restrict__ aim,
                                                       const uint16_t* __restrict__ cr, const uint16_t* __restrict__ ci,
                                                       const uint16_t* __restrict__ ldt, const uint16_t* __restrict__ dsk,
                                                       const uint16_t* __restrict__ w1, const uint16_t* __restrict__ w2,
                                                       char* ws, uint16_t* __restrict__ out) {
    extern __shared__ __attribute__((aligned(16))) char lds[];
    const int b = blockIdx.x;
    if (b < NFFN) ffn_role(u, w1, w2, ws, out, lds, b);
    else ssm_role(u, lar, aim, cr, ci, ldt, dsk, ws, lds, b - NFFN);
}

extern "C" void kernel_launch(void* const* d_in, const int* in_sizes, int n_in,
                              void* d_out, int out_size, void* d_ws, size_t ws_size,
                              hipStream_t stream) {
    (void)hipFuncSetAttribute((const void*)fused_kernel, hipFuncAttributeMaxDynamicSharedMemorySize, LDS_BYTES);
    fused_kernel<<<NFFN + NSSM, 512, LDS_BYTES, stream>>>((const uint16_t*)d_in[0], (const uint16_t*)d_in[1], (const uint16_t*)d_in[2], (const uint16_t*)d_in[3],
                                                  (const uint16_t*)d_in[4], (const uint16_t*)d_in[5], (const uint16_t*)d_in[6], (const uint16_t*)d_in[8],
                                                  (const uint16_t*)d_in[9], (char*)d_ws, (uint16_t*)d_out);
}
```

```cpp
#include <hip/hip_runtime.h>
#include <stdint.h>

#define L_ 2048
#define D_ 448
#define N_ 256
#define F_ 1792

typedef __attribute__((ext_vector_type(8))) __bf16 bf8;
typedef __attribute__((ext_vector_type(8))) _Float16 h8;
typedef __attribute__((ext_vector_type(4))) short s4v;
typedef __attribute__((ext_vector_type(4))) float f4;
typedef __attribute__((ext_vector_type(2))) float f2;
typedef __attribute__((ext_vector_type(4))) unsigned u4;
typedef __attribute__((ext_vector_type(2))) unsigned u2;
typedef __attribute__((ext_vector_type(2))) __bf16 bf2v;
typedef __attribute__((ext_vector_type(2))) _Float16 h2v;
typedef unsigned long long u64;
#define LAS __attribute__((address_space(3)))
#define GAS __attribute__((address_space(1)))
typedef GAS u64 gu64;
#define RLX_AGENT __ATOMIC_RELAXED, __HIP_MEMORY_SCOPE_AGENT

__device__ __forceinline__ float bf2f(uint16_t v) { return __uint_as_float(((uint32_t)v) << 16); }
__device__ __forceinline__ unsigned pk_bf16(float a, float b) { bf2v r; r.x = (__bf16)a; r.y = (__bf16)b; return __builtin_bit_cast(unsigned, r); }
__device__ __forceinline__ unsigned pk_f16(float a, float b) { h2v r; r.x = (_Float16)a; r.y = (_Float16)b; return __builtin_bit_cast(unsigned, r); }
__device__ __forceinline__ float rbf(float f) { return __uint_as_float(pk_bf16(f, 0.f) << 16); }
__device__ __forceinline__ float lo_f(unsigned w) { return __uint_as_float(w << 16); }
__device__ __forceinline__ float hi_f(unsigned w) { return __uint_as_float(w & 0xffff0000u); }
__device__ __forceinline__ float lo_h(unsigned w) { return (float)__builtin_bit_cast(h2v, w).x; }
__device__ __forceinline__ float hi_h(unsigned w) { return (float)__builtin_bit_cast(h2v, w).y; }
__device__ __forceinline__ f4 mfma_bf(u4 a, u4 b, f4 c) { return __builtin_amdgcn_mfma_f32_16x16x32_bf16(__builtin_bit_cast(bf8, a), __builtin_bit_cast(bf8, b), c, 0, 0, 0); }
__device__ __forceinline__ f4 mfma_h(u4 a, u4 b, f4 c) { return __builtin_amdgcn_mfma_f32_16x16x32_f16(__builtin_bit_cast(h8, a), __builtin_bit_cast(h8, b), c, 0, 0, 0); }
template <int CTRL> __device__ __forceinline__ float dpp_f(float v) { return __int_as_float(__builtin_amdgcn_update_dpp(0, __float_as_int(v), CTRL, 0xf, 0xf, false)); }
__device__ __forceinline__ float row16_sum(float v) { v += dpp_f<0x128>(v); v += dpp_f<0x124>(v); v += dpp_f<0x122>(v); v += dpp_f<0x121>(v); return v; }
__device__ __forceinline__ float row16_max(float v) { v = fmaxf(v, dpp_f<0x128>(v)); v = fmaxf(v, dpp_f<0x124>(v)); v = fmaxf(v, dpp_f<0x122>(v)); v = fmaxf(v, dpp_f<0x121>(v)); return v; }
__device__ __forceinline__ u2 tr1(const char* p) { return __builtin_bit_cast(u2, __builtin_amdgcn_ds_read_tr16_b64_v4i16((LAS s4v*)p)); }
__device__ __forceinline__ void dma_pair(unsigned dst, unsigned va, unsigned vb, const void* sbase) {
    asm volatile("s_mov_b32 m0, %0\n\ts_nop 0\n\tglobal_load_lds_dwordx4 %1, %3\n\tglobal_load_lds_dwordx4 %2, %3 offset:1024" :: "s"(dst), "v"(va), "v"(vb), "s"(sbase) : "memory");
}
#define WAIT_VM(n) asm volatile("s_waitcnt vmcnt(" #n ")" ::: "memory")
#define WAIT_LGKM0() asm volatile("s_waitcnt lgkmcnt(0)" ::: "memory")
#define RAW_BAR() do { WAIT_LGKM0(); __builtin_amdgcn_s_barrier(); asm volatile("" ::: "memory"); } while (0)

#define TRDECL
#define TR(i)
#define TRFLUSH
#define TRW 0
#define SLEEPN 4

#define XTM 4
#define ROTM 1
#define NFFN 128
#define NSSM 112
#define WS_EPOCH 0
#define NREP 16
#define WS_FX    65536
#define WS_ST    32768
#define WS_FS    131072
#define WS_FP    196608
#define WS_PSC   262144
#define WS_XT    (1u << 20)
#define WS_S     (3u << 20)
#define WS_PART  (5u << 20)
#define WS_BYTES (16u << 20)
#define LDS_BYTES 160000
#define SPIN_CAP (1u << 14)

__device__ __forceinline__ void wait_flags(const gu64* p, bool active, u64 tag) {
    for (unsigned spins = 0; spins < SPIN_CAP; ++spins) {
        u64 v = active ? __hip_atomic_load(p, RLX_AGENT) : tag;
        if (__all(v == tag)) break;
        __builtin_amdgcn_s_sleep(SLEEPN);
    }
    __builtin_amdgcn_fence(__ATOMIC_ACQUIRE, "wavefront");
}

#define XN_BYTES 57344
#define RING_OFF XN_BYTES
#define RSL 7
#define RING_W (RSL * 2048)
#define ST_OFF (RING_OFF + 7 * RING_W)
__device__ __forceinline__ void ffn_role(const uint16_t* __restrict__ u, const uint16_t* __restrict__ w1, const uint16_t* __restrict__ w2,
                                         char* ws, uint16_t* __restrict__ out, char* lds, int b) {
    const int tid = threadIdx.x, lane = tid & 63, wave = __builtin_amdgcn_readfirstlane(tid >> 6);
    const int r16 = lane & 15, g = lane >> 4;
    const int xcd = b & 7, ks = xcd & 3, rt = 16 * (xcd >> 2) + (b >> 3);
    const int rot = ROTM * ((b >> 3) % 14);
    TRDECL;
    TR(0);
    const __amdgpu_buffer_rsrc_t rs = __builtin_amdgcn_make_buffer_rsrc(ws, 0, WS_BYTES, 0x00020000);
    u4 ur[2][4];
    {
        const u4* up = (const u4*)(u + (size_t)(64 * rt + 8 * wave + g) * D_) + (r16 < 14 ? r16 : 13);
        const u4* up2 = up + 4 * 56;
        asm volatile("global_load_dwordx4 %0, %8, off\n\tglobal_load_dwordx4 %1, %8, off offset:224\n\tglobal_load_dwordx4 %2, %8, off offset:448\n\tglobal_load_dwordx4 %3, %8, off offset:672\n\t"
                     "global_load_dwordx4 %4, %9, off\n\tglobal_load_dwordx4 %5, %9, off offset:224\n\tglobal_load_dwordx4 %6, %9, off offset:448\n\tglobal_load_dwordx4 %7, %9, off offset:672"
                     : "=&v"(ur[0][0]), "=&v"(ur[0][1]), "=&v"(ur[0][2]), "=&v"(ur[0][3]), "=&v"(ur[1][0]), "=&v"(ur[1][1]), "=&v"(ur[1][2]), "=&v"(ur[1][3])
                     : "v"(up), "v"(up2) : "memory");
    }
    const unsigned ldsbase = (unsigned)(size_t)(LAS char*)lds;
    const unsigned ring = ldsbase + RING_OFF + wave * RING_W;
    const int rho = lane >> 3;
    const unsigned srcc = (unsigned)(((lane & 7) ^ (((rho >> 1) & 3) << 1)) * 16);
    const int krow = 8 * (rho >> 2) + (rho & 3);
    const unsigned v1a = krow * (F_ * 2) + srcc, v1b = v1a + 16 * F_ * 2 - 1024;
    const unsigned v2a = krow * (D_ * 2) + srcc, v2b = v2a + 16 * D_ * 2 - 1024;
    const char* w1b = (const char*)(w1 + 448 * ks + 64 * wave);
    const char* w2b = (const char*)(w2 + (size_t)448 * ks * D_ + 64 * wave);
#define KROT(i) ((i) + rot >= 14 ? (i) + rot - 14 : (i) + rot)
#define ISSUE(s) do { if ((s) < 28) dma_pair(ring + ((s) % RSL) * 2048, v1a, v1b, w1b + (size_t)KROT((s) >> 1) * (32 * F_ * 2) + ((s) & 1) * (4 * F_ * 2)); \
                      else if ((s) < 56) dma_pair(ring + ((s) % RSL) * 2048, v2a, v2b, w2b + (size_t)KROT(((s) - 28) >> 1) * (32 * D_ * 2) + ((s) & 1) * (4 * D_ * 2)); } while (0)
    u64 tag = 0;
    if (wave < 7) { ISSUE(0); ISSUE(1); }
    else WAIT_VM(0);
    TR(9);
    asm volatile("s_waitcnt vmcnt(4)" : "+v"(ur[0][0]), "+v"(ur[0][1]), "+v"(ur[0][2]), "+v"(ur[0][3]), "+v"(ur[1][0]), "+v"(ur[1][1]), "+v"(ur[1][2]), "+v"(ur[1][3]) :: "memory");
    TR(10);
    if (wave == 7) { __builtin_amdgcn_s_setprio(3); tag = *(const u64*)(ws + WS_EPOCH) + 1ull; }
#pragma unroll
    for (int p = 0; p < 2; ++p) {
        const bool act = r16 < 14;
        const bf2v one2 = {(__bf16)1.0f, (__bf16)1.0f};
        float s = 0.f;
#pragma unroll
        for (int i = 0; i < 4; ++i)
#pragma unroll
            for (int e = 0; e < 4; ++e) { const unsigned w_ = ur[p][i][e]; s = __builtin_amdgcn_fdot2_f32_bf16(__builtin_bit_cast(bf2v, w_), one2, s, false); }
        s = row16_sum(act ? s : 0.f);
        const float mean = s * (1.0f / D_);
        const float mb = rbf(mean);
        unsigned av[4][4];
        f2 q2 = {0.f, 0.f};
#pragma unroll
        for (int i = 0; i < 4; ++i)
#pragma unroll
            for (int e = 0; e < 4; ++e) {
                f2 d = (f2){lo_f(ur[p][i][e]), hi_f(ur[p][i][e])} - (f2){mb, mb};
                q2 = __builtin_elementwise_fma(d, d, q2);
                av[i][e] = pk_bf16(d.x, d.y);
            }
        const float q = row16_sum(act ? q2.x + q2.y : 0.f);
        const float dm = mean - mb;
        const float var = fmaxf(fmaf(-dm, dm, q * (1.0f / D_)), 0.f);
        const float sd = rbf(__builtin_amdgcn_sqrtf(rbf(rbf(var) + 1e-5f)));
        const float rinv = __builtin_amdgcn_rcpf(sd);
        const int lrow = 8 * wave + 4 * p + g;
        if (r16 == 0) *(unsigned*)(lds + ST_OFF + 4 * lrow) = (__float_as_uint(mb) >> 16) | (__float_as_uint(sd) & 0xffff0000u);
        char* xr = lds + lrow * 896;
        const int sw = (lrow >> 1) & 7;
#pragma unroll
        for (int i = 0; i < 4; ++i) {
            u4 o;
#pragma unroll
            for (int e = 0; e < 4; ++e) {
                f2 r = (f2){lo_f(av[i][e]), hi_f(av[i][e])} * (f2){rinv, rinv};
                o[e] = pk_bf16(r.x, r.y);
            }
            if (act) *(u4*)(xr + 16 * ((r16 + 14 * i) ^ sw)) = o;
        }
    }
    if (wave < 7) { ISSUE(2); ISSUE(3); ISSUE(4); ISSUE(5); ISSUE(6); }
    if (tid == 0) *(unsigned*)(lds + ST_OFF + 256) = 0u;
    TR(1);
    RAW_BAR();
    TR(2);
    const int q4 = r16 >> 2, p4 = lane & 3;
    if (wave == 7) {
        if (ks == 0) {
            __builtin_amdgcn_raw_buffer_store_b32(*(const unsigned*)(lds + ST_OFF + 4 * lane), rs, WS_ST + (64 * rt + lane) * 4, 0, 16);
            WAIT_VM(0);
            if (lane < NREP) __hip_atomic_store((gu64*)(ws + WS_FX) + lane * 64 + rt, tag, RLX_AGENT);
        }
        {
            const u4 ones = {0xffffffffu, 0xffffffffu, 0xffffffffu, 0xffffffffu};
            const unsigned pb = WS_PART + (unsigned)((rt * 4 + ks) * 56) * 1024 + lane * 16;
            for (int i = 0; i < 56; ++i) if (((i >> 1) & 3) != ks) __builtin_amdgcn_raw_buffer_store_b128(ones, rs, pb + i * 1024, 0, 16);
            __builtin_amdgcn_raw_buffer_store_b128(ones, rs, WS_PSC + (rt * 4 + ks) * 2048 + lane * 16, 0, 16);
            __builtin_amdgcn_raw_buffer_store_b128(ones, rs, WS_PSC + (rt * 4 + ks) * 2048 + 1024 + lane * 16, 0, 16);
            WAIT_VM(0);
            if (lane == 0) __hip_atomic_store((gu64*)(ws + WS_FP) + rt * 16 + ks, tag, RLX_AGENT);
        }
        __builtin_amdgcn_s_setprio(0);
        RAW_BAR();
        RAW_BAR();
        wait_flags((const gu64*)(ws + WS_FS) + (rt & (NREP - 1)) * 128 + lane, true, tag);
        {
            const gu64* p = lane < 48 ? (const gu64*)(ws + WS_FS) + (rt & (NREP - 1)) * 128 + 64 + lane : (const gu64*)(ws + WS_FP) + rt * 16 + (lane & 3);
            wait_flags(p, lane < 52, tag);
        }
        if (lane == 0) __hip_atomic_store((LAS unsigned*)((LAS char*)lds + ST_OFF + 256), 1u, __ATOMIC_RELAXED, __HIP_MEMORY_SCOPE_WORKGROUP);
        if (b == 0) {
#pragma unroll
            for (int i = 0; i < 2; ++i) wait_flags((const gu64*)(ws + WS_FP) + (16 * i + (lane >> 2)) * 16 + (lane & 3), true, tag);
            if (lane == 0) __hip_atomic_store((gu64*)(ws + WS_EPOCH), tag, RLX_AGENT);
        }
        return;
    }
    const int rr = 4 * g + q4;
    const int wtr = rr * 128 + (((rr >> 1) & 3) << 5) + 8 * p4;
    const char* rpb = lds + RING_OFF + wave * RING_W;
    const char* rp0 = rpb + wtr; const char* rp1 = rpb + (wtr ^ 32); const char* rp2 = rpb + (wtr ^ 64); const char* rp3 = rpb + (wtr ^ 96);
    const int asw = (r16 >> 1) & 7;
    const unsigned ae = (unsigned)((g ^ asw) * 16);
    const char* a0p = lds + r16 * 896;
    f4 acc[4][4];
#pragma unroll
    for (int m = 0; m < 4; ++m)
#pragma unroll
        for (int n = 0; n < 4; ++n) acc[m][n] = (f4){0, 0, 0, 0};
    u4 af[2][4]; u2 x[2][2][4];
#define VMW(n) do { if ((n) >= 10) WAIT_VM(10); else if ((n) >= 8) WAIT_VM(8); else if ((n) >= 6) WAIT_VM(6); else if ((n) >= 4) WAIT_VM(4); else if ((n) >= 2) WAIT_VM(2); else WAIT_VM(0); } while (0)
#define LOADW(j, bsel, issued) do { VMW(2 * ((issued) - (2 * (j) + 1))); \
        _Pragma("unroll") for (int hf = 0; hf < 2; ++hf) { const int so = ((2 * (j) + hf) % RSL) * 2048; \
            x[bsel][hf][0] = tr1(rp0 + so); x[bsel][hf][1] = tr1(rp1 + so); x[bsel][hf][2] = tr1(rp2 + so); x[bsel][hf][3] = tr1(rp3 + so); } } while (0)
#define LOADA(it, bsel) do { const char* ap = a0p + (ae ^ (unsigned)(KROT(it) << 6)); \
        _Pragma("unroll") for (int m = 0; m < 4; ++m) af[bsel][m] = *(const u4*)(ap + m * 16 * 896); } while (0)
#define REFILL(j) do { WAIT_LGKM0(); ISSUE(2 * (j) + RSL); ISSUE(2 * (j) + 1 + RSL); } while (0)
#define ISSUED(j) ((2 * (j) + 1 + RSL) < 55 ? (2 * (j) + 1 + RSL) : 55)
    LOADW(0, 0, RSL - 1); LOADA(0, 0);
    REFILL(0);
#pragma unroll
    for (int it = 0; it < 14; ++it) {
        const int cur = it & 1, nxt = cur ^ 1;
        LOADW(it + 1, nxt, ISSUED(it));
        if (it < 13) LOADA(it + 1, nxt);
        __builtin_amdgcn_sched_barrier(0);
#pragma unroll
        for (int n = 0; n < 4; ++n) {
            const u4 wf = {x[cur][0][n].x, x[cur][0][n].y, x[cur][1][n].x, x[cur][1][n].y};
#pragma unroll
            for (int m = 0; m < 4; ++m) acc[m][n] = mfma_bf(wf, af[cur][m], acc[m][n]);
            __builtin_amdgcn_sched_barrier(0);
            if (n == 1) { WAIT_LGKM0(); ISSUE(2 * (it + 1) + RSL); }
            if (n == 2) { ISSUE(2 * (it + 1) + 1 + RSL); }
            __builtin_amdgcn_sched_barrier(0);
        }
    }
    TR(3);
    {
        u2 hp[4][4];
#pragma unroll
        for (int m = 0; m < 4; ++m)
#pragma unroll
            for (int n = 0; n < 4; ++n) {
                typedef __attribute__((ext_vector_type(2))) short s2v;
                const unsigned p0 = pk_bf16(acc[m][n][0], acc[m][n][1]), p1 = pk_bf16(acc[m][n][2], acc[m][n][3]);
                hp[m][n] = (u2){__builtin_bit_cast(unsigned, __builtin_elementwise_max(__builtin_bit_cast(s2v, p0), (s2v){0, 0})), __builtin_bit_cast(unsigned, __builtin_elementwise_max(__builtin_bit_cast(s2v, p1), (s2v){0, 0}))};
                acc[m][n] = (f4){0, 0, 0, 0};
            }
        __builtin_amdgcn_sched_barrier(0);
        RAW_BAR();
#pragma unroll
        for (int m = 0; m < 4; ++m)
#pragma unroll
            for (int n = 0; n < 4; ++n)
                *(u2*)(lds + (16 * m + r16) * 896 + 16 * ((8 * wave + 2 * n + (g >> 1)) ^ asw) + 8 * (g & 1)) = hp[m][n];
    }
    RAW_BAR();
    TR(4);
    LOADA(0, 0);
#pragma unroll
    for (int it = 0; it < 14; ++it) {
        const int cur = it & 1, nxt = cur ^ 1;
        if (it < 13) { LOADW(14 + it + 1, nxt, ISSUED(14 + it)); LOADA(it + 1, nxt); }
        __builtin_amdgcn_sched_barrier(0);
#pragma unroll
        for (int n = 0; n < 4; ++n) {
            const u4 wf = {x[cur][0][n].x, x[cur][0][n].y, x[cur][1][n].x, x[cur][1][n].y};
#pragma unroll
            for (int m = 0; m < 4; ++m) acc[m][n] = mfma_bf(wf, af[cur][m], acc[m][n]);
            __builtin_amdgcn_sched_barrier(0);
            if (n == 1 && 2 * (14 + it + 1) + RSL < 56) { WAIT_LGKM0(); ISSUE(2 * (14 + it + 1) + RSL); }
            if (n == 2 && 2 * (14 + it + 1) + RSL < 56) { ISSUE(2 * (14 + it + 1) + 1 + RSL); }
            __builtin_amdgcn_sched_barrier(0);
        }
    }
    TR(5);
    {
        float mx = 0.f;
#pragma unroll
        for (int m = 0; m < 4; ++m)
#pragma unroll
            for (int n = 0; n < 4; ++n)
#pragma unroll
                for (int e = 0; e < 4; ++e) mx = fmaxf(mx, fabsf(acc[m][n][e]));
        int e2 = (int)((__float_as_uint(mx) >> 23) & 255u) - 126;
        e2 = e2 < -100 ? -100 : e2;
        const float sc = __uint_as_float((unsigned)(127 + 15 - e2) << 23);
        const float scinv = __uint_as_float((unsigned)(127 - 15 + e2) << 23);
        const unsigned pbase = WS_PART + (unsigned)(((rt * 4 + ks) * 7 + wave) * 8) * 1024 + lane * 16;
#pragma unroll
        for (int m = 0; m < 4; ++m) {
            if (m != ks) {
#pragma unroll
                for (int np = 0; np < 2; ++np) {
                    u4 o = {pk_f16(acc[m][2 * np][0] * sc, acc[m][2 * np][1] * sc), pk_f16(acc[m][2 * np][2] * sc, acc[m][2 * np][3] * sc),
                            pk_f16(acc[m][2 * np + 1][0] * sc, acc[m][2 * np + 1][1] * sc), pk_f16(acc[m][2 * np + 1][2] * sc, acc[m][2 * np + 1][3] * sc)};
#pragma unroll
                    for (int e = 0; e < 4; ++e) o[e] = o[e] < 0xfffffffeu ? o[e] : 0xfffffffeu;
                    __builtin_amdgcn_raw_buffer_store_b128(o, rs, pbase + (2 * m + np) * 1024, 0, 16);
                }
            }
        }
        __builtin_amdgcn_raw_buffer_store_b32(__float_as_uint(scinv), rs, WS_PSC + (((rt * 4 + ks) * 8 + wave) * 64 + lane) * 4, 0, 16);
    }
    f4 own[4];
#pragma unroll
    for (int n = 0; n < 4; ++n) own[n] = ks == 0 ? acc[0][n] : ks == 1 ? acc[1][n] : ks == 2 ? acc[2][n] : acc[3][n];
    TR(6);
    const int trow = 64 * rt + 16 * ks;
    const size_t uo = (size_t)(trow + (lane >> 3)) * D_ + 64 * wave + 8 * (lane & 7);
    const u4 uu0 = *(const u4*)(u + uo), uu1 = *(const u4*)(u + uo + 8 * D_);
    for (unsigned spins = 0; spins < (1u << 20); ++spins) {
        if (__hip_atomic_load((LAS unsigned*)((LAS char*)lds + ST_OFF + 256), __ATOMIC_RELAXED, __HIP_MEMORY_SCOPE_WORKGROUP)) break;
        __builtin_amdgcn_s_sleep(2);
    }
    asm volatile("" ::: "memory");
    TR(7);
    {
        u2 sv[4];
#pragma unroll
        for (int n = 0; n < 4; ++n) sv[n] = __builtin_amdgcn_raw_buffer_load_b64(rs, WS_S + (16 * wave + 4 * n + g) * 16384 + (trow + r16) * 8, 0, 16);
        u4 pv[3][2]; float psc[3];
        for (unsigned spins = 0; spins < SPIN_CAP; ++spins) {
            unsigned mxv = 0;
#pragma unroll
            for (int j = 0; j < 3; ++j) {
                const int kp = (ks + 1 + j) & 3;
#pragma unroll
                for (int np = 0; np < 2; ++np)
                    pv[j][np] = __builtin_amdgcn_raw_buffer_load_b128(rs, WS_PART + (unsigned)((((rt * 4 + kp) * 7 + wave) * 8) + 2 * ks + np) * 1024 + lane * 16, 0, 16);
                psc[j] = __uint_as_float(__builtin_amdgcn_raw_buffer_load_b32(rs, WS_PSC + (((rt * 4 + kp) * 8 + wave) * 64 + lane) * 4, 0, 16));
            }
#pragma unroll
            for (int j = 0; j < 3; ++j) {
#pragma unroll
                for (int np = 0; np < 2; ++np)
#pragma unroll
                    for (int e = 0; e < 4; ++e) { const unsigned w_ = pv[j][np][e]; mxv = mxv > w_ ? mxv : w_; }
                const unsigned w_ = __float_as_uint(psc[j]); mxv = mxv > w_ ? mxv : w_;
            }
            if (!__any(mxv == 0xffffffffu)) break;
            __builtin_amdgcn_s_sleep(1);
            asm volatile("" ::: "memory");
        }
        TR(11);
#pragma unroll
        for (int j = 0; j < 3; ++j)
#pragma unroll
            for (int n = 0; n < 4; ++n) {
                const unsigned w0 = pv[j][n >> 1][2 * (n & 1)], w1_ = pv[j][n >> 1][2 * (n & 1) + 1];
                own[n][0] = fmaf(lo_h(w0), psc[j], own[n][0]); own[n][1] = fmaf(hi_h(w0), psc[j], own[n][1]);
                own[n][2] = fmaf(lo_h(w1_), psc[j], own[n][2]); own[n][3] = fmaf(hi_h(w1_), psc[j], own[n][3]);
            }
        char* tb = lds + RING_OFF + wave * RING_W;
#pragma unroll
        for (int n = 0; n < 4; ++n) {
            float sg[4];
#pragma unroll
            for (int e = 0; e < 4; ++e) {
                const unsigned sw_ = sv[n][e >> 1];
                float s = (e & 1) ? hi_f(sw_) : lo_f(sw_);
                float o = rbf(own[n][e]);
                float gg = rbf(o * __builtin_amdgcn_rcpf(1.f + __expf(-o)));
                sg[e] = s * gg;
            }
            *(u2*)(tb + r16 * 144 + 32 * n + 8 * g) = (u2){pk_bf16(sg[0], sg[1]), pk_bf16(sg[2], sg[3])};
        }
#pragma unroll
        for (int h = 0; h < 2; ++h) {
            const u4 sgv = *(const u4*)(tb + (8 * h + (lane >> 3)) * 144 + 16 * (lane & 7));
            const u4 uv = h ? uu1 : uu0;
            u4 o;
#pragma unroll
            for (int e = 0; e < 4; ++e) { const unsigned a = uv[e], c = sgv[e]; o[e] = pk_bf16(lo_f(a) + lo_f(c), hi_f(a) + hi_f(c)); }
            *(u4*)(out + uo + (size_t)h * 8 * D_) = o;
        }
    }
    TR(8);
    TRFLUSH;
}

__device__ __forceinline__ f2 cmul(f2 a, f2 b) {
    f2 t = a * b.xx;
    f2 by = b.yy; by.x = -by.x;
    return __builtin_elementwise_fma(a.yx, by, t);
}
__device__ __forceinline__ f2 zpow(float a2, float wv, float p) {
    float e = __builtin_amdgcn_exp2f(a2 * p);
    float th = __builtin_amdgcn_fractf(wv * p);
    return (f2){e * __builtin_amdgcn_cosf(th), e * __builtin_amdgcn_sinf(th)};
}
struct c2 { f2 a, b; };
__device__ __forceinline__ c2 cmul2(c2 x, c2 y) { c2 o; o.a = cmul(x.a, y.a); o.b = cmul(x.b, y.b); return o; }
__device__ __forceinline__ u2 pkc(c2 v) { return (u2){pk_f16(v.a.x, v.a.y), pk_f16(v.b.x, v.b.y)}; }
__device__ __forceinline__ u2 pkcn(c2 v) { return (u2){pk_f16(v.a.x, -v.a.y), pk_f16(v.b.x, -v.b.y)}; }
#define KS_ROW 528
#define KS_WAVE (16 * KS_ROW)
#define SSM_KS   0
#define SSM_XC   67584
#define SSM_KR   134144
#define SSM_KD   152576
#define SSM_BD   157824
#define SSM_RED  0

__device__ __forceinline__ void ssm_role(const uint16_t* __restrict__ u, const uint16_t* __restrict__ lar, const uint16_t* __restrict__ aim,
                                         const uint16_t* __restrict__ cr, const uint16_t* __restrict__ ci,
                                         const uint16_t* __restrict__ ldt, const uint16_t* __restrict__ dsk,
                                         char* ws, char* lds, int sb) {
    const int tid = threadIdx.x, lane = tid & 63, wave = __builtin_amdgcn_readfirstlane(tid >> 6);
    const int r16 = lane & 15, g = lane >> 4;
    const int c0 = 4 * sb;
    const int ch = wave >> 1;
    TRDECL;
    TR(0);
    u64 tag = 0;
    const __amdgpu_buffer_rsrc_t rs = __builtin_amdgcn_make_buffer_rsrc(ws, 0, WS_BYTES, 0x00020000);
    u2 ucol[4];
    {
        const int c = c0 + ch, n0 = c * N_ + 128 * (wave & 1) + 2 * lane;
        const unsigned wl = *(const unsigned*)(lar + n0), wa = *(const unsigned*)(aim + n0), wr = *(const unsigned*)(cr + n0), wi = *(const unsigned*)(ci + n0);
        const uint16_t wdt = ldt[c];
        if (wave == 7) tag = *(const u64*)(ws + WS_EPOCH) + 1ull;
        const float dt = rbf(expf(bf2f(wdt)));
        const float ar0 = -rbf(expf(lo_f(wl))) * dt, ar1 = -rbf(expf(hi_f(wl))) * dt;
        const float ai0 = lo_f(wa) * dt, ai1 = hi_f(wa) * dt;
        const float Cr0 = lo_f(wr) * dt, Cr1 = hi_f(wr) * dt, Ci0 = lo_f(wi) * dt, Ci1 = hi_f(wi) * dt;
        float bd = row16_sum((fabsf(Cr0) + fabsf(Ci0)) + (fabsf(Cr1) + fabsf(Ci1)));
        bd += __shfl_xor(bd, 16); bd += __shfl_xor(bd, 32);
        TR(1);
        int eb = (int)((__float_as_uint(bd) >> 23) & 255u) - 126;
        eb = eb < -100 ? -100 : eb;
        const float S1 = __uint_as_float((unsigned)(127 + 14 - eb) << 23);
        const float S1inv = __uint_as_float((unsigned)(127 + eb - 27) << 23);
        const float a20 = ar0 * 1.44269504088896f, wv0 = ai0 * 0.159154943091895f, a21 = ar1 * 1.44269504088896f, wv1 = ai1 * 0.159154943091895f;
        char* sc = lds + SSM_KS + wave * KS_WAVE;
        char* wp = sc + 8 * lane;
        const char* rp = sc + r16 * KS_ROW + 16 * g;
#define ZP(p) (c2){zpow(a20, wv0, p), zpow(a21, wv1, p)}
        c2 z1 = ZP(1.f), z8 = ZP(8.f), z64 = ZP(64.f), z128 = ZP(128.f), z1024 = ZP(1024.f);
#define WR(e, v) *(u2*)(wp + (e) * KS_ROW) = (v)
        u4 bfr[8];
        {
            c2 cj = {(f2){16384.f, 0.f}, (f2){16384.f, 0.f}};
#pragma unroll
            for (int j = 0; j < 8; ++j) {
                c2 hi = cmul2(cj, z64);
                WR(j, pkc(cj)); WR(8 + j, pkc(hi));
                if (j < 7) cj = cmul2(cj, z8);
            }
#pragma unroll
            for (int k4 = 0; k4 < 8; ++k4) bfr[k4] = *(const u4*)(rp + 64 * k4);
        }
        asm volatile("" :: "v"(bfr[0]) : "memory");
#pragma unroll
        for (int e = 0; e < 4; ++e) ucol[e] = *(const u2*)(u + (size_t)(4 * tid + e) * D_ + c0);
        c2 ze[8];
        ze[1] = z1; ze[2] = cmul2(z1, z1); ze[3] = cmul2(ze[2], z1); ze[4] = cmul2(ze[2], ze[2]);
        ze[5] = cmul2(ze[4], z1); ze[6] = cmul2(ze[4], ze[2]); ze[7] = cmul2(ze[4], ze[3]);
        c2 b = {(f2){Cr0 * S1, Ci0 * S1}, (f2){Cr1 * S1, Ci1 * S1}};
        c2 bh = cmul2(b, z1024);
        f4 acc[3];
#pragma unroll
        for (int i = 0; i < 2; ++i) {
            WR(8 * i, pkcn(b));
#pragma unroll
            for (int e = 1; e < 8; ++e) WR(8 * i + e, pkcn(cmul2(ze[e], b)));
            b = cmul2(b, z128);
        }
        acc[0] = (f4){0, 0, 0, 0};
#pragma unroll
        for (int k4 = 0; k4 < 8; ++k4) acc[0] = mfma_h(*(const u4*)(rp + 64 * k4), bfr[k4], acc[0]);
#pragma unroll
        for (int i = 0; i < 2; ++i) {
            WR(4 * i, pkcn(b));
#pragma unroll
            for (int e = 1; e < 4; ++e) WR(4 * i + e, pkcn(cmul2(ze[2 * e], b)));
            b = cmul2(b, z128);
        }
#pragma unroll
        for (int i = 0; i < 4; ++i) {
            WR(8 + 2 * i, pkcn(b));
            WR(8 + 2 * i + 1, pkcn(cmul2(b, ze[4])));
            if (i < 3) b = cmul2(b, z128);
        }
        acc[1] = (f4){0, 0, 0, 0};
#pragma unroll
        for (int k4 = 0; k4 < 8; ++k4) acc[1] = mfma_h(*(const u4*)(rp + 64 * k4), bfr[k4], acc[1]);
#pragma unroll
        for (int i = 0; i < 9; ++i) {
            WR(i, pkcn(bh));
            if (i < 8) bh = cmul2(bh, z128);
        }
        acc[2] = (f4){0, 0, 0, 0};
#pragma unroll
        for (int k4 = 0; k4 < 8; ++k4) acc[2] = mfma_h(*(const u4*)(rp + 64 * k4), bfr[k4], acc[2]);
        float* pd = (float*)sc;
        *(f4*)(pd + 128 * (g >> 1) + 4 * (g & 1) + 8 * r16) = acc[0] * S1inv;
        if (g < 2) *(f4*)(pd + 256 + 64 * g + 4 * r16) = acc[1] * S1inv;
        else {
            *(f2*)(pd + 384 + 64 * (g - 2) + 2 * r16) = (f2){acc[1][0], acc[1][1]} * S1inv;
            *(f2*)(pd + 384 + 64 * (g - 2) + 32 + 2 * r16) = (f2){acc[1][2], acc[1][3]} * S1inv;
        }
        if (g < 3) {
#pragma unroll
            for (int e = 0; e < 4; ++e) pd[512 + 16 * (4 * g + e) + r16] = acc[2][e] * S1inv;
        }
        if (lane == 0) *(float*)(lds + SSM_BD + 4 * wave) = bd;
    }
    TR(2);
    __syncthreads();
    float SKinv[4];
    {
        const float* bdp = (const float*)(lds + SSM_BD);
        float SKs[4];
#pragma unroll
        for (int c = 0; c < 4; ++c) {
            float bb = 2.f * (bdp[2 * c] + bdp[2 * c + 1]);
            int e0 = (int)((__float_as_uint(bb) >> 23) & 255u) - 126; e0 = e0 < -100 ? -100 : e0;
            SKinv[c] = __uint_as_float((unsigned)(127 - 15 + e0) << 23); SKs[c] = __uint_as_float((unsigned)(127 + 15 - e0) << 23);
        }
        const int c = tid >> 7, q = tid & 127;
        const float SK = c == 0 ? SKs[0] : c == 1 ? SKs[1] : c == 2 ? SKs[2] : SKs[3];
#pragma unroll
        for (int rep = 0; rep < 2; ++rep) {
            const int qq = q + 128 * rep;
            if (qq < 164) {
                const char* s0 = lds + SSM_KS + (2 * c) * KS_WAVE + qq * 16;
                f4 v = (*(const f4*)s0 + *(const f4*)(s0 + KS_WAVE)) * SK;
                *(u2*)(lds + SSM_KD + c * 1312 + qq * 8) = (u2){pk_f16(v[0], v[1]), pk_f16(v[2], v[3])};
            }
        }
    }
    __syncthreads();
    TR(3);
    {
        const int xa_i = tid & 255;
        int idx[8];
#pragma unroll
        for (int j = 0; j < 8; j += 2) {
            int l0 = 2047 - (8 * xa_i + j), l1 = l0 - 1;
            unsigned bits = pk_bf16((float)l0, (float)l1);
            idx[j] = l0 < 128 ? l0 : (int)((bits & 0xffffu) - 0x4280u);
            idx[j + 1] = l1 < 128 ? l1 : (int)((bits >> 16) - 0x4280u);
        }
#pragma unroll
        for (int rep = 0; rep < 2; ++rep) {
            const int c = (tid >> 8) + 2 * rep;
            const uint16_t* kd = (const uint16_t*)(lds + SSM_KD + c * 1312);
            u4 o;
#pragma unroll
            for (int j = 0; j < 4; ++j) o[j] = (unsigned)kd[idx[2 * j]] | ((unsigned)kd[idx[2 * j + 1]] << 16);
            char* kr = lds + SSM_KR + c * 4608;
            *(u4*)(kr + 16 * xa_i) = o;
            if (xa_i < 32) *(u4*)(kr + 4096 + 16 * xa_i) = (u4){0, 0, 0, 0};
        }
        if (tid < 128) { *(u2*)(lds + SSM_XC + (tid >> 5) * 16640 + ((tid >> 3) & 3) * 4160 + 8 * (tid & 7)) = (u2){0, 0}; }
    }
    if (wave == 7) wait_flags((const gu64*)(ws + WS_FX) + (sb & (NREP - 1)) * 64 + (lane & 31), true, tag);
    __syncthreads();
    TR(4);
    {
        const u4 st = __builtin_amdgcn_raw_buffer_load_b128(rs, WS_ST + tid * 16, 0, 16);
        float xv[4][4];
#pragma unroll
        for (int e = 0; e < 4; ++e) {
            const unsigned sw_ = st[e];
            const float mb = lo_f(sw_), rinv = __builtin_amdgcn_rcpf(hi_f(sw_));
#pragma unroll
            for (int h = 0; h < 2; ++h) {
                const unsigned uw = ucol[e][h];
                unsigned a = pk_bf16(lo_f(uw) - mb, hi_f(uw) - mb);
                unsigned r = pk_bf16(lo_f(a) * rinv, hi_f(a) * rinv);
                xv[2 * h][e] = lo_f(r); xv[2 * h + 1][e] = hi_f(r);
            }
        }
#pragma unroll
        for (int cc = 0; cc < 4; ++cc) {
            const unsigned d0 = pk_f16(xv[cc][0], xv[cc][1]), d1 = pk_f16(xv[cc][2], xv[cc][3]);
            const unsigned n0 = __shfl_down(d0, 1), n1 = __shfl_down(d1, 1);
            const unsigned mid = __builtin_amdgcn_alignbyte(d1, d0, 2), e1 = __builtin_amdgcn_alignbyte(n0, d1, 2), e2 = __builtin_amdgcn_alignbyte(n1, n0, 2);
            char* xc = lds + SSM_XC + cc * 16640 + 8 * (tid + 8);
            *(u2*)(xc) = (u2){d0, d1};
            if (lane < 63) {
                *(u2*)(xc + 4160) = (u2){mid, e1};
                *(u2*)(xc + 2 * 4160) = (u2){d1, n0};
                *(u2*)(xc + 3 * 4160) = (u2){e1, e2};
            } else {
                *(unsigned*)(xc + 4160) = mid; *(uint16_t*)(xc + 4160 + 4) = (uint16_t)(d1 >> 16);
                *(unsigned*)(xc + 2 * 4160) = d1;
                *(uint16_t*)(xc + 3 * 4160) = (uint16_t)(d1 >> 16);
            }
            if (lane == 0) {
                *(uint16_t*)(xc + 4160 - 2) = (uint16_t)d0;
                *(unsigned*)(xc + 2 * 4160 - 4) = d0;
                *(uint16_t*)(xc + 3 * 4160 - 6) = (uint16_t)d0; *(unsigned*)(xc + 3 * 4160 - 4) = mid;
            }
        }
    }
    __syncthreads();
    TR(5);
    f4 acc[8];
#pragma unroll
    for (int n = 0; n < 8; ++n) acc[n] = (f4){0, 0, 0, 0};
    {
        const int rh = wave & 1;
        const char* xc = lds + SSM_XC + ch * 16640 + ((r16 + 1) & 3) * 4160 + 8 * (10 - 2 * g + ((r16 + 1) >> 2)) + 64 * 4 * rh;
        const char* kr = lds + SSM_KR + ch * 4608 + 16 * (257 - 2 * r16 - g) + 64 * 4 * rh;
#pragma unroll
        for (int rr = 0; rr < 4; ++rr) {
            u4 af[8], bfv[8];
#pragma unroll
            for (int kp = 0; kp < 8; ++kp) {
                const char* p = xc + 64 * (8 * kp + rr);
                u2 a = *(const u2*)p, b = *(const u2*)(p + 8);
                af[kp] = (u4){a.x, a.y, b.x, b.y};
            }
#pragma unroll
            for (int j = 0; j < 8; ++j) bfv[j] = *(const u4*)(kr - 64 * (8 * j - rr));
#pragma unroll
            for (int n = 0; n < 8; ++n)
#pragma unroll
                for (int kp = 0; kp <= n; ++kp) acc[n] = mfma_h(af[kp], bfv[n - kp], acc[n]);
        }
    }
    TR(6);
    {
        char* red = lds + SSM_RED;
#pragma unroll
        for (int n = 0; n < 8; ++n) *(f4*)(red + ((wave * 8 + n) * 64 + lane) * 16) = acc[n];
    }
    __syncthreads();
    TR(7);
    {
        const char* red = lds + SSM_RED;
        const int t = 256 * wave + 16 * r16 + 4 * g;
        float s[4][4];
#pragma unroll
        for (int c = 0; c < 4; ++c) {
            f4 y = *(const f4*)(red + (((2 * c) * 8 + wave) * 64 + lane) * 16) + *(const f4*)(red + (((2 * c + 1) * 8 + wave) * 64 + lane) * 16);
            const u2 xx = *(const u2*)(lds + SSM_XC + c * 16640 + 2 * (t + 32));
            const float ds = bf2f(dsk[c0 + c]);
#pragma unroll
            for (int e = 0; e < 4; ++e) {
                const unsigned xw = xx[e >> 1];
                float x = (e & 1) ? hi_h(xw) : lo_h(xw);
                s[c][e] = rbf(y[e] * SKinv[c]) + rbf(x * ds);
            }
        }
        const unsigned soff = WS_S + sb * 16384 + t * 8;
        __builtin_amdgcn_raw_buffer_store_b128((u4){pk_bf16(s[0][0], s[1][0]), pk_bf16(s[2][0], s[3][0]), pk_bf16(s[0][1], s[1][1]), pk_bf16(s[2][1], s[3][1])}, rs, soff, 0, 16);
        __builtin_amdgcn_raw_buffer_store_b128((u4){pk_bf16(s[0][2], s[1][2]), pk_bf16(s[2][2], s[3][2]), pk_bf16(s[0][3], s[1][3]), pk_bf16(s[2][3], s[3][3])}, rs, soff + 16, 0, 16);
    }
    WAIT_VM(0);
    __syncthreads();
    if (tid >= 448 && tid < 448 + NREP) __hip_atomic_store((gu64*)(ws + WS_FS) + (tid - 448) * 128 + sb, tag, RLX_AGENT);
    TR(8);
    TRFLUSH;
}

__global__ __launch_bounds__(512, 2) void fused_kernel(const uint16_t* __restrict__ u, const uint16_t* __restrict__ lar, const uint16_t* __restrict__ aim,
                                                       const uint16_t* __restrict__ cr, const uint16_t* __restrict__ ci,
                                                       const uint16_t* __restrict__ ldt, const uint16_t* __restrict__ dsk,
                                                       const uint16_t* __restrict__ w1, const uint16_t* __restrict__ w2,
                                                       char* ws, uint16_t* __restrict__ out) {
    extern __shared__ __attribute__((aligned(16))) char lds[];
    const int b = blockIdx.x;
    if (b < NFFN) ffn_role(u, w1, w2, ws, out, lds, b);
    else ssm_role(u, lar, aim, cr, ci, ldt, dsk, ws, lds, b - NFFN);
}

extern "C" void kernel_launch(void* const* d_in, const int* in_sizes, int n_in,
                              void* d_out, int out_size, void* d_ws, size_t ws_size,
                              hipStream_t stream) {
    (void)hipFuncSetAttribute((const void*)fused_kernel, hipFuncAttributeMaxDynamicSharedMemorySize, LDS_BYTES);
    fused_kernel<<<NFFN + NSSM, 512, LDS_BYTES, stream>>>((const uint16_t*)d_in[0], (const uint16_t*)d_in[1], (const uint16_t*)d_in[2], (const uint16_t*)d_in[3],
                                                  (const uint16_t*)d_in[4], (const uint16_t*)d_in[5], (const uint16_t*)d_in[6], (const uint16_t*)d_in[8],
                                                  (const uint16_t*)d_in[9], (char*)d_ws, (uint16_t*)d_out);
}
```

```cpp
#include <hip/hip_runtime.h>
#include <stdint.h>

#define L_ 2048
#define D_ 448
#define N_ 256
#define F_ 1792

typedef __attribute__((ext_vector_type(8))) __bf16 bf8;
typedef __attribute__((ext_vector_type(8))) _Float16 h8;
typedef __attribute__((ext_vector_type(4))) short s4v;
typedef __attribute__((ext_vector_type(4))) float f4;
typedef __attribute__((ext_vector_type(2))) float f2;
typedef __attribute__((ext_vector_type(4))) unsigned u4;
typedef __attribute__((ext_vector_type(2))) unsigned u2;
typedef __attribute__((ext_vector_type(2))) __bf16 bf2v;
typedef __attribute__((ext_vector_type(2))) _Float16 h2v;
typedef unsigned long long u64;
#define LAS __attribute__((address_space(3)))
#define GAS __attribute__((address_space(1)))
typedef GAS u64 gu64;
#define RLX_AGENT __ATOMIC_RELAXED, __HIP_MEMORY_SCOPE_AGENT

__device__ __forceinline__ float bf2f(uint16_t v) { return __uint_as_float(((uint32_t)v) << 16); }
__device__ __forceinline__ unsigned pk_bf16(float a, float b) { bf2v r; r.x = (__bf16)a; r.y = (__bf16)b; return __builtin_bit_cast(unsigned, r); }
__device__ __forceinline__ unsigned pk_f16(float a, float b) { h2v r; r.x = (_Float16)a; r.y = (_Float16)b; return __builtin_bit_cast(unsigned, r); }
__device__ __forceinline__ float rbf(float f) { return __uint_as_float(pk_bf16(f, 0.f) << 16); }
__device__ __forceinline__ float lo_f(unsigned w) { return __uint_as_float(w << 16); }
__device__ __forceinline__ float hi_f(unsigned w) { return __uint_as_float(w & 0xffff0000u); }
__device__ __forceinline__ float lo_h(unsigned w) { return (float)__builtin_bit_cast(h2v, w).x; }
__device__ __forceinline__ float hi_h(unsigned w) { return (float)__builtin_bit_cast(h2v, w).y; }
__device__ __forceinline__ f4 mfma_bf(u4 a, u4 b, f4 c) { return __builtin_amdgcn_mfma_f32_16x16x32_bf16(__builtin_bit_cast(bf8, a), __builtin_bit_cast(bf8, b), c, 0, 0, 0); }
__device__ __forceinline__ f4 mfma_h(u4 a, u4 b, f4 c) { return __builtin_amdgcn_mfma_f32_16x16x32_f16(__builtin_bit_cast(h8, a), __builtin_bit_cast(h8, b), c, 0, 0, 0); }
template <int CTRL> __device__ __forceinline__ float dpp_f(float v) { return __int_as_float(__builtin_amdgcn_update_dpp(0, __float_as_int(v), CTRL, 0xf, 0xf, false)); }
__device__ __forceinline__ float row16_sum(float v) { v += dpp_f<0x128>(v); v += dpp_f<0x124>(v); v += dpp_f<0x122>(v); v += dpp_f<0x121>(v); return v; }
__device__ __forceinline__ float row16_max(float v) { v = fmaxf(v, dpp_f<0x128>(v)); v = fmaxf(v, dpp_f<0x124>(v)); v = fmaxf(v, dpp_f<0x122>(v)); v = fmaxf(v, dpp_f<0x121>(v)); return v; }
__device__ __forceinline__ u2 tr1(const char* p) { return __builtin_bit_cast(u2, __builtin_amdgcn_ds_read_tr16_b64_v4i16((LAS s4v*)p)); }
__device__ __forceinline__ void dma_pair(unsigned dst, unsigned va, unsigned vb, const void* sbase) {
    asm volatile("s_mov_b32 m0, %0\n\ts_nop 0\n\tglobal_load_lds_dwordx4 %1, %3\n\tglobal_load_lds_dwordx4 %2, %3 offset:1024" :: "s"(dst), "v"(va), "v"(vb), "s"(sbase) : "memory");
}
#define WAIT_VM(n) asm volatile("s_waitcnt vmcnt(" #n ")" ::: "memory")
#define WAIT_LGKM0() asm volatile("s_waitcnt lgkmcnt(0)" ::: "memory")
#define RAW_BAR() do { WAIT_LGKM0(); __builtin_amdgcn_s_barrier(); asm volatile("" ::: "memory"); } while (0)

#define TRDECL
#define TR(i)
#define TRFLUSH
#define TRW 0
#define SLEEPN 4

#define XTM 4
#define ROTM 1
#define NFFN 128
#define NSSM 112
#define WS_EPOCH 0
#define NREP 16
#define WS_FX    65536
#define WS_ST    32768
#define WS_FS    131072
#define WS_FP    196608
#define WS_PSC   262144
#define WS_XT    (1u << 20)
#define WS_S     (3u << 20)
#define WS_PART  (5u << 20)
#define WS_BYTES (16u << 20)
#define LDS_BYTES 160000
#define SPIN_CAP (1u << 14)

__device__ __forceinline__ void wait_flags(const gu64* p, bool active, u64 tag) {
    for (unsigned spins = 0; spins < SPIN_CAP; ++spins) {
        u64 v = active ? __hip_atomic_load(p, RLX_AGENT) : tag;
        if (__all(v == tag)) break;
        __builtin_amdgcn_s_sleep(SLEEPN);
    }
    __builtin_amdgcn_fence(__ATOMIC_ACQUIRE, "wavefront");
}

#define XN_BYTES 57344
#define RING_OFF XN_BYTES
#define RSL 7
#define RING_W (RSL * 2048)
#define ST_OFF (RING_OFF + 7 * RING_W)
__device__ __forceinline__ void ffn_role(const uint16_t* __restrict__ u, const uint16_t* __restrict__ w1, const uint16_t* __restrict__ w2,
                                         char* ws, uint16_t* __restrict__ out, char* lds, int b) {
    const int tid = threadIdx.x, lane = tid & 63, wave = __builtin_amdgcn_readfirstlane(tid >> 6);
    const int r16 = lane & 15, g = lane >> 4;
    const int xcd = b & 7, ks = xcd & 3, rt = 16 * (xcd >> 2) + (b >> 3);
    const int rot = ROTM * ((b >> 3) % 14);
    TRDECL;
    TR(0);
    const __amdgpu_buffer_rsrc_t rs = __builtin_amdgcn_make_buffer_rsrc(ws, 0, WS_BYTES, 0x00020000);
    u4 ur[2][4];
    {
        const u4* up = (const u4*)(u + (size_t)(64 * rt + 8 * wave + g) * D_) + (r16 < 14 ? r16 : 13);
        const u4* up2 = up + 4 * 56;
        asm volatile("global_load_dwordx4 %0, %8, off\n\tglobal_load_dwordx4 %1, %8, off offset:224\n\tglobal_load_dwordx4 %2, %8, off offset:448\n\tglobal_load_dwordx4 %3, %8, off offset:672\n\t"
                     "global_load_dwordx4 %4, %9, off\n\tglobal_load_dwordx4 %5, %9, off offset:224\n\tglobal_load_dwordx4 %6, %9, off offset:448\n\tglobal_load_dwordx4 %7, %9, off offset:672"
                     : "=&v"(ur[0][0]), "=&v"(ur[0][1]), "=&v"(ur[0][2]), "=&v"(ur[0][3]), "=&v"(ur[1][0]), "=&v"(ur[1][1]), "=&v"(ur[1][2]), "=&v"(ur[1][3])
                     : "v"(up), "v"(up2) : "memory");
    }
    const unsigned ldsbase = (unsigned)(size_t)(LAS char*)lds;
    const unsigned ring = ldsbase + RING_OFF + wave * RING_W;
    const int rho = lane >> 3;
    const unsigned srcc = (unsigned)(((lane & 7) ^ (((rho >> 1) & 3) << 1)) * 16);
    const int krow = 8 * (rho >> 2) + (rho & 3);
    const unsigned v1a = krow * (F_ * 2) + srcc, v1b = v1a + 16 * F_ * 2 - 1024;
    const unsigned v2a = krow * (D_ * 2) + srcc, v2b = v2a + 16 * D_ * 2 - 1024;
    const char* w1b = (const char*)(w1 + 448 * ks + 64 * wave);
    const char* w2b = (const char*)(w2 + (size_t)448 * ks * D_ + 64 * wave);
#define KROT(i) ((i) + rot >= 14 ? (i) + rot - 14 : (i) + rot)
#define ISSUE(s) do { if ((s) < 28) dma_pair(ring + ((s) % RSL) * 2048, v1a, v1b, w1b + (size_t)KROT((s) >> 1) * (32 * F_ * 2) + ((s) & 1) * (4 * F_ * 2)); \
                      else if ((s) < 56) dma_pair(ring + ((s) % RSL) * 2048, v2a, v2b, w2b + (size_t)KROT(((s) - 28) >> 1) * (32 * D_ * 2) + ((s) & 1) * (4 * D_ * 2)); } while (0)
    u64 tag = 0;
    if (wave < 7) { ISSUE(0); ISSUE(1); ISSUE(2); ISSUE(3); }
    else WAIT_VM(0);
    TR(9);
    asm volatile("s_waitcnt vmcnt(8)" : "+v"(ur[0][0]), "+v"(ur[0][1]), "+v"(ur[0][2]), "+v"(ur[0][3]), "+v"(ur[1][0]), "+v"(ur[1][1]), "+v"(ur[1][2]), "+v"(ur[1][3]) :: "memory");
    TR(10);
    if (wave == 7) { __builtin_amdgcn_s_setprio(3); tag = *(const u64*)(ws + WS_EPOCH) + 1ull; }
#pragma unroll
    for (int p = 0; p < 2; ++p) {
        const bool act = r16 < 14;
        const bf2v one2 = {(__bf16)1.0f, (__bf16)1.0f};
        float s = 0.f;
#pragma unroll
        for (int i = 0; i < 4; ++i)
#pragma unroll
            for (int e = 0; e < 4; ++e) { const unsigned w_ = ur[p][i][e]; s = __builtin_amdgcn_fdot2_f32_bf16(__builtin_bit_cast(bf2v, w_), one2, s, false); }
        s = row16_sum(act ? s : 0.f);
        const float mean = s * (1.0f / D_);
        const float mb = rbf(mean);
        unsigned av[4][4];
        f2 q2 = {0.f, 0.f};
#pragma unroll
        for (int i = 0; i < 4; ++i)
#pragma unroll
            for (int e = 0; e < 4; ++e) {
                f2 d = (f2){lo_f(ur[p][i][e]), hi_f(ur[p][i][e])} - (f2){mb, mb};
                q2 = __builtin_elementwise_fma(d, d, q2);
                av[i][e] = pk_bf16(d.x, d.y);
            }
#pragma unroll
        for (int i = 0; i < 4; ++i) asm volatile("" : "+v"(av[i][0]), "+v"(av[i][1]), "+v"(av[i][2]), "+v"(av[i][3]));
        const float q = row16_sum(act ? q2.x + q2.y : 0.f);
        const float dm = mean - mb;
        const float var = fmaxf(fmaf(-dm, dm, q * (1.0f / D_)), 0.f);
        const float sd = rbf(__builtin_amdgcn_sqrtf(rbf(rbf(var) + 1e-5f)));
        const float rinv = __builtin_amdgcn_rcpf(sd);
        const int lrow = 8 * wave + 4 * p + g;
        if (r16 == 0) *(unsigned*)(lds + ST_OFF + 4 * lrow) = (__float_as_uint(mb) >> 16) | (__float_as_uint(sd) & 0xffff0000u);
        char* xr = lds + lrow * 896;
        const int sw = (lrow >> 1) & 7;
#pragma unroll
        for (int i = 0; i < 4; ++i) {
            u4 o;
#pragma unroll
            for (int e = 0; e < 4; ++e) {
                f2 r = (f2){lo_f(av[i][e]), hi_f(av[i][e])} * (f2){rinv, rinv};
                o[e] = pk_bf16(r.x, r.y);
            }
            if (act) *(u4*)(xr + 16 * ((r16 + 14 * i) ^ sw)) = o;
        }
    }
    if (wave < 7) { ISSUE(4); ISSUE(5); ISSUE(6); }
    if (tid == 0) *(unsigned*)(lds + ST_OFF + 256) = 0u;
    TR(1);
    RAW_BAR();
    TR(2);
    const int q4 = r16 >> 2, p4 = lane & 3;
    if (wave == 7) {
        if (ks == 0) {
            __builtin_amdgcn_raw_buffer_store_b32(*(const unsigned*)(lds + ST_OFF + 4 * lane), rs, WS_ST + (64 * rt + lane) * 4, 0, 16);
            WAIT_VM(0);
            if (lane < NREP) __hip_atomic_store((gu64*)(ws + WS_FX) + lane * 64 + rt, tag, RLX_AGENT);
        }
        {
            const u4 ones = {0xffffffffu, 0xffffffffu, 0xffffffffu, 0xffffffffu};
            const unsigned pb = WS_PART + (unsigned)((rt * 4 + ks) * 56) * 1024 + lane * 16;
            for (int i = 0; i < 56; ++i) if (((i >> 1) & 3) != ks) __builtin_amdgcn_raw_buffer_store_b128(ones, rs, pb + i * 1024, 0, 16);
            __builtin_amdgcn_raw_buffer_store_b128(ones, rs, WS_PSC + (rt * 4 + ks) * 2048 + lane * 16, 0, 16);
            __builtin_amdgcn_raw_buffer_store_b128(ones, rs, WS_PSC + (rt * 4 + ks) * 2048 + 1024 + lane * 16, 0, 16);
            WAIT_VM(0);
            if (lane == 0) __hip_atomic_store((gu64*)(ws + WS_FP) + rt * 16 + ks, tag, RLX_AGENT);
        }
        __builtin_amdgcn_s_setprio(0);
        RAW_BAR();
        RAW_BAR();
        wait_flags((const gu64*)(ws + WS_FS) + (rt & (NREP - 1)) * 128 + lane, true, tag);
        {
            const gu64* p = lane < 48 ? (const gu64*)(ws + WS_FS) + (rt & (NREP - 1)) * 128 + 64 + lane : (const gu64*)(ws + WS_FP) + rt * 16 + (lane & 3);
            wait_flags(p, lane < 52, tag);
        }
        if (lane == 0) __hip_atomic_store((LAS unsigned*)((LAS char*)lds + ST_OFF + 256), 1u, __ATOMIC_RELAXED, __HIP_MEMORY_SCOPE_WORKGROUP);
        if (b == 0) {
#pragma unroll
            for (int i = 0; i < 2; ++i) wait_flags((const gu64*)(ws + WS_FP) + (16 * i + (lane >> 2)) * 16 + (lane & 3), true, tag);
            if (lane == 0) __hip_atomic_store((gu64*)(ws + WS_EPOCH), tag, RLX_AGENT);
        }
        return;
    }
    const int rr = 4 * g + q4;
    const int wtr = rr * 128 + (((rr >> 1) & 3) << 5) + 8 * p4;
    const char* rpb = lds + RING_OFF + wave * RING_W;
    const char* rp0 = rpb + wtr; const char* rp1 = rpb + (wtr ^ 32); const char* rp2 = rpb + (wtr ^ 64); const char* rp3 = rpb + (wtr ^ 96);
    const int asw = (r16 >> 1) & 7;
    const unsigned ae = (unsigned)((g ^ asw) * 16);
    const char* a0p = lds + r16 * 896;
    f4 acc[4][4];
#pragma unroll
    for (int m = 0; m < 4; ++m)
#pragma unroll
        for (int n = 0; n < 4; ++n) acc[m][n] = (f4){0, 0, 0, 0};
    u4 af[2][4]; u2 x[2][2][4];
#define VMW(n) do { if ((n) >= 10) WAIT_VM(10); else if ((n) >= 8) WAIT_VM(8); else if ((n) >= 6) WAIT_VM(6); else if ((n) >= 4) WAIT_VM(4); else if ((n) >= 2) WAIT_VM(2); else WAIT_VM(0); } while (0)
#define LOADW(j, bsel, issued) do { VMW(2 * ((issued) - (2 * (j) + 1))); \
        _Pragma("unroll") for (int hf = 0; hf < 2; ++hf) { const int so = ((2 * (j) + hf) % RSL) * 2048; \
            x[bsel][hf][0] = tr1(rp0 + so); x[bsel][hf][1] = tr1(rp1 + so); x[bsel][hf][2] = tr1(rp2 + so); x[bsel][hf][3] = tr1(rp3 + so); } } while (0)
#define LOADA(it, bsel) do { const char* ap = a0p + (ae ^ (unsigned)(KROT(it) << 6)); \
        _Pragma("unroll") for (int m = 0; m < 4; ++m) af[bsel][m] = *(const u4*)(ap + m * 16 * 896); } while (0)
#define REFILL(j) do { WAIT_LGKM0(); ISSUE(2 * (j) + RSL); ISSUE(2 * (j) + 1 + RSL); } while (0)
#define ISSUED(j) ((2 * (j) + 1 + RSL) < 55 ? (2 * (j) + 1 + RSL) : 55)
    LOADW(0, 0, RSL - 1); LOADA(0, 0);
    REFILL(0);
#pragma unroll
    for (int it = 0; it < 14; ++it) {
        const int cur = it & 1, nxt = cur ^ 1;
        LOADW(it + 1, nxt, ISSUED(it));
        if (it < 13) LOADA(it + 1, nxt);
        __builtin_amdgcn_sched_barrier(0);
#pragma unroll
        for (int n = 0; n < 4; ++n) {
            const u4 wf = {x[cur][0][n].x, x[cur][0][n].y, x[cur][1][n].x, x[cur][1][n].y};
#pragma unroll
            for (int m = 0; m < 4; ++m) acc[m][n] = mfma_bf(wf, af[cur][m], acc[m][n]);
            __builtin_amdgcn_sched_barrier(0);
            if (n == 1) { WAIT_LGKM0(); ISSUE(2 * (it + 1) + RSL); }
            if (n == 2) { ISSUE(2 * (it + 1) + 1 + RSL); }
            __builtin_amdgcn_sched_barrier(0);
        }
    }
    TR(3);
    {
        u2 hp[4][4];
#pragma unroll
        for (int m = 0; m < 4; ++m)
#pragma unroll
            for (int n = 0; n < 4; ++n) {
                typedef __attribute__((ext_vector_type(2))) short s2v;
                const unsigned p0 = pk_bf16(acc[m][n][0], acc[m][n][1]), p1 = pk_bf16(acc[m][n][2], acc[m][n][3]);
                hp[m][n] = (u2){__builtin_bit_cast(unsigned, __builtin_elementwise_max(__builtin_bit_cast(s2v, p0), (s2v){0, 0})), __builtin_bit_cast(unsigned, __builtin_elementwise_max(__builtin_bit_cast(s2v, p1), (s2v){0, 0}))};
                acc[m][n] = (f4){0, 0, 0, 0};
            }
        __builtin_amdgcn_sched_barrier(0);
        RAW_BAR();
#pragma unroll
        for (int m = 0; m < 4; ++m)
#pragma unroll
            for (int n = 0; n < 4; ++n)
                *(u2*)(lds + (16 * m + r16) * 896 + 16 * ((8 * wave + 2 * n + (g >> 1)) ^ asw) + 8 * (g & 1)) = hp[m][n];
    }
    RAW_BAR();
    TR(4);
    LOADA(0, 0);
#pragma unroll
    for (int it = 0; it < 14; ++it) {
        const int cur = it & 1, nxt = cur ^ 1;
        if (it < 13) { LOADW(14 + it + 1, nxt, ISSUED(14 + it)); LOADA(it + 1, nxt); }
        __builtin_amdgcn_sched_barrier(0);
#pragma unroll
        for (int n = 0; n < 4; ++n) {
            const u4 wf = {x[cur][0][n].x, x[cur][0][n].y, x[cur][1][n].x, x[cur][1][n].y};
#pragma unroll
            for (int m = 0; m < 4; ++m) acc[m][n] = mfma_bf(wf, af[cur][m], acc[m][n]);
            __builtin_amdgcn_sched_barrier(0);
            if (n == 1 && 2 * (14 + it + 1) + RSL < 56) { WAIT_LGKM0(); ISSUE(2 * (14 + it + 1) + RSL); }
            if (n == 2 && 2 * (14 + it + 1) + RSL < 56) { ISSUE(2 * (14 + it + 1) + 1 + RSL); }
            __builtin_amdgcn_sched_barrier(0);
        }
    }
    TR(5);
    {
        float mx = 0.f;
#pragma unroll
        for (int m = 0; m < 4; ++m)
#pragma unroll
            for (int n = 0; n < 4; ++n)
#pragma unroll
                for (int e = 0; e < 4; ++e) mx = fmaxf(mx, fabsf(acc[m][n][e]));
        int e2 = (int)((__float_as_uint(mx) >> 23) & 255u) - 126;
        e2 = e2 < -100 ? -100 : e2;
        const float sc = __uint_as_float((unsigned)(127 + 15 - e2) << 23);
        const float scinv = __uint_as_float((unsigned)(127 - 15 + e2) << 23);
        const unsigned pbase = WS_PART + (unsigned)(((rt * 4 + ks) * 7 + wave) * 8) * 1024 + lane * 16;
#pragma unroll
        for (int m = 0; m < 4; ++m) {
            if (m != ks) {
#pragma unroll
                for (int np = 0; np < 2; ++np) {
                    u4 o = {pk_f16(acc[m][2 * np][0] * sc, acc[m][2 * np][1] * sc), pk_f16(acc[m][2 * np][2] * sc, acc[m][2 * np][3] * sc),
                            pk_f16(acc[m][2 * np + 1][0] * sc, acc[m][2 * np + 1][1] * sc), pk_f16(acc[m][2 * np + 1][2] * sc, acc[m][2 * np + 1][3] * sc)};
#pragma unroll
                    for (int e = 0; e < 4; ++e) o[e] = o[e] < 0xfffffffeu ? o[e] : 0xfffffffeu;
                    __builtin_amdgcn_raw_buffer_store_b128(o, rs, pbase + (2 * m + np) * 1024, 0, 16);
                }
            }
        }
        __builtin_amdgcn_raw_buffer_store_b32(__float_as_uint(scinv), rs, WS_PSC + (((rt * 4 + ks) * 8 + wave) * 64 + lane) * 4, 0, 16);
    }
    f4 own[4];
#pragma unroll
    for (int n = 0; n < 4; ++n) own[n] = ks == 0 ? acc[0][n] : ks == 1 ? acc[1][n] : ks == 2 ? acc[2][n] : acc[3][n];
    TR(6);
    const int trow = 64 * rt + 16 * ks;
    const size_t uo = (size_t)(trow + (lane >> 3)) * D_ + 64 * wave + 8 * (lane & 7);
    const u4 uu0 = *(const u4*)(u + uo), uu1 = *(const u4*)(u + uo + 8 * D_);
    for (unsigned spins = 0; spins < (1u << 20); ++spins) {
        if (__hip_atomic_load((LAS unsigned*)((LAS char*)lds + ST_OFF + 256), __ATOMIC_RELAXED, __HIP_MEMORY_SCOPE_WORKGROUP)) break;
        __builtin_amdgcn_s_sleep(2);
    }
    asm volatile("" ::: "memory");
    TR(7);
    {
        u2 sv[4];
#pragma unroll
        for (int n = 0; n < 4; ++n) sv[n] = __builtin_amdgcn_raw_buffer_load_b64(rs, WS_S + (16 * wave + 4 * n + g) * 16384 + (trow + r16) * 8, 0, 16);
        u4 pv[3][2]; float psc[3];
        for (unsigned spins = 0; spins < SPIN_CAP; ++spins) {
            unsigned mxv = 0;
#pragma unroll
            for (int j = 0; j < 3; ++j) {
                const int kp = (ks + 1 + j) & 3;
#pragma unroll
                for (int np = 0; np < 2; ++np)
                    pv[j][np] = __builtin_amdgcn_raw_buffer_load_b128(rs, WS_PART + (unsigned)((((rt * 4 + kp) * 7 + wave) * 8) + 2 * ks + np) * 1024 + lane * 16, 0, 16);
                psc[j] = __uint_as_float(__builtin_amdgcn_raw_buffer_load_b32(rs, WS_PSC + (((rt * 4 + kp) * 8 + wave) * 64 + lane) * 4, 0, 16));
            }
#pragma unroll
            for (int j = 0; j < 3; ++j) {
#pragma unroll
                for (int np = 0; np < 2; ++np)
#pragma unroll
                    for (int e = 0; e < 4; ++e) { const unsigned w_ = pv[j][np][e]; mxv = mxv > w_ ? mxv : w_; }
                const unsigned w_ = __float_as_uint(psc[j]); mxv = mxv > w_ ? mxv : w_;
            }
            if (!__any(mxv == 0xffffffffu)) break;
            __builtin_amdgcn_s_sleep(1);
            asm volatile("" ::: "memory");
        }
        TR(11);
#pragma unroll
        for (int j = 0; j < 3; ++j)
#pragma unroll
            for (int n = 0; n < 4; ++n) {
                const unsigned w0 = pv[j][n >> 1][2 * (n & 1)], w1_ = pv[j][n >> 1][2 * (n & 1) + 1];
                own[n][0] = fmaf(lo_h(w0), psc[j], own[n][0]); own[n][1] = fmaf(hi_h(w0), psc[j], own[n][1]);
                own[n][2] = fmaf(lo_h(w1_), psc[j], own[n][2]); own[n][3] = fmaf(hi_h(w1_), psc[j], own[n][3]);
            }
        char* tb = lds + RING_OFF + wave * RING_W;
#pragma unroll
        for (int n = 0; n < 4; ++n) {
            float sg[4];
#pragma unroll
            for (int e = 0; e < 4; ++e) {
                const unsigned sw_ = sv[n][e >> 1];
                float s = (e & 1) ? hi_f(sw_) : lo_f(sw_);
                float o = rbf(own[n][e]);
                float gg = rbf(o * __builtin_amdgcn_rcpf(1.f + __expf(-o)));
                sg[e] = s * gg;
            }
            *(u2*)(tb + r16 * 144 + 32 * n + 8 * g) = (u2){pk_bf16(sg[0], sg[1]), pk_bf16(sg[2], sg[3])};
        }
#pragma unroll
        for (int h = 0; h < 2; ++h) {
            const u4 sgv = *(const u4*)(tb + (8 * h + (lane >> 3)) * 144 + 16 * (lane & 7));
            const u4 uv = h ? uu1 : uu0;
            u4 o;
#pragma unroll
            for (int e = 0; e < 4; ++e) { const unsigned a = uv[e], c = sgv[e]; o[e] = pk_bf16(lo_f(a) + lo_f(c), hi_f(a) + hi_f(c)); }
            *(u4*)(out + uo + (size_t)h * 8 * D_) = o;
        }
    }
    TR(8);
    TRFLUSH;
}

__device__ __forceinline__ f2 cmul(f2 a, f2 b) {
    f2 t = a * b.xx;
    f2 by = b.yy; by.x = -by.x;
    return __builtin_elementwise_fma(a.yx, by, t);
}
__device__ __forceinline__ f2 zpow(float a2, float wv, float p) {
    float e = __builtin_amdgcn_exp2f(a2 * p);
    float th = __builtin_amdgcn_fractf(wv * p);
    return (f2){e * __builtin_amdgcn_cosf(th), e * __builtin_amdgcn_sinf(th)};
}
struct c2 { f2 a, b; };
__device__ __forceinline__ c2 cmul2(c2 x, c2 y) { c2 o; o.a = cmul(x.a, y.a); o.b = cmul(x.b, y.b); return o; }
__device__ __forceinline__ u2 pkc(c2 v) { return (u2){pk_f16(v.a.x, v.a.y), pk_f16(v.b.x, v.b.y)}; }
__device__ __forceinline__ u2 pkcn(c2 v) { return (u2){pk_f16(v.a.x, -v.a.y), pk_f16(v.b.x, -v.b.y)}; }
#define KS_ROW 528
#define KS_WAVE (16 * KS_ROW)
#define SSM_KS   0
#define SSM_XC   67584
#define SSM_KR   134144
#define SSM_KD   152576
#define SSM_BD   157824
#define SSM_RED  0

__device__ __forceinline__ void ssm_role(const uint16_t* __restrict__ u, const uint16_t* __restrict__ lar, const uint16_t* __restrict__ aim,
                                         const uint16_t* __restrict__ cr, const uint16_t* __restrict__ ci,
                                         const uint16_t* __restrict__ ldt, const uint16_t* __restrict__ dsk,
                                         char* ws, char* lds, int sb) {
    const int tid = threadIdx.x, lane = tid & 63, wave = __builtin_amdgcn_readfirstlane(tid >> 6);
    const int r16 = lane & 15, g = lane >> 4;
    const int c0 = 4 * sb;
    const int ch = wave >> 1;
    TRDECL;
    TR(0);
    u64 tag = 0;
    const __amdgpu_buffer_rsrc_t rs = __builtin_amdgcn_make_buffer_rsrc(ws, 0, WS_BYTES, 0x00020000);
    u2 ucol[4];
    {
        const int c = c0 + ch, n0 = c * N_ + 128 * (wave & 1) + 2 * lane;
        const unsigned wl = *(const unsigned*)(lar + n0), wa = *(const unsigned*)(aim + n0), wr = *(const unsigned*)(cr + n0), wi = *(const unsigned*)(ci + n0);
        const uint16_t wdt = ldt[c];
        if (wave == 7) tag = *(const u64*)(ws + WS_EPOCH) + 1ull;
        const float dt = rbf(expf(bf2f(wdt)));
        const float ar0 = -rbf(expf(lo_f(wl))) * dt, ar1 = -rbf(expf(hi_f(wl))) * dt;
        const float ai0 = lo_f(wa) * dt, ai1 = hi_f(wa) * dt;
        const float Cr0 = lo_f(wr) * dt, Cr1 = hi_f(wr) * dt, Ci0 = lo_f(wi) * dt, Ci1 = hi_f(wi) * dt;
        float bd = row16_sum((fabsf(Cr0) + fabsf(Ci0)) + (fabsf(Cr1) + fabsf(Ci1)));
        bd += __shfl_xor(bd, 16); bd += __shfl_xor(bd, 32);
        TR(1);
        int eb = (int)((__float_as_uint(bd) >> 23) & 255u) - 126;
        eb = eb < -100 ? -100 : eb;
        const float S1 = __uint_as_float((unsigned)(127 + 14 - eb) << 23);
        const float S1inv = __uint_as_float((unsigned)(127 + eb - 27) << 23);
        const float a20 = ar0 * 1.44269504088896f, wv0 = ai0 * 0.159154943091895f, a21 = ar1 * 1.44269504088896f, wv1 = ai1 * 0.159154943091895f;
        char* sc = lds + SSM_KS + wave * KS_WAVE;
        char* wp = sc + 8 * lane;
        const char* rp = sc + r16 * KS_ROW + 16 * g;
#define ZP(p) (c2){zpow(a20, wv0, p), zpow(a21, wv1, p)}
        c2 z1 = ZP(1.f), z8 = ZP(8.f), z64 = ZP(64.f), z128 = ZP(128.f), z1024 = ZP(1024.f);
#define WR(e, v) *(u2*)(wp + (e) * KS_ROW) = (v)
        u4 bfr[8];
        {
            c2 cj = {(f2){16384.f, 0.f}, (f2){16384.f, 0.f}};
#pragma unroll
            for (int j = 0; j < 8; ++j) {
                c2 hi = cmul2(cj, z64);
                WR(j, pkc(cj)); WR(8 + j, pkc(hi));
                if (j < 7) cj = cmul2(cj, z8);
            }
#pragma unroll
            for (int k4 = 0; k4 < 8; ++k4) bfr[k4] = *(const u4*)(rp + 64 * k4);
        }
        asm volatile("" :: "v"(bfr[0]) : "memory");
#pragma unroll
        for (int e = 0; e < 4; ++e) ucol[e] = *(const u2*)(u + (size_t)(4 * tid + e) * D_ + c0);
        c2 ze[8];
        ze[1] = z1; ze[2] = cmul2(z1, z1); ze[3] = cmul2(ze[2], z1); ze[4] = cmul2(ze[2], ze[2]);
        ze[5] = cmul2(ze[4], z1); ze[6] = cmul2(ze[4], ze[2]); ze[7] = cmul2(ze[4], ze[3]);
        c2 b = {(f2){Cr0 * S1, Ci0 * S1}, (f2){Cr1 * S1, Ci1 * S1}};
        c2 bh = cmul2(b, z1024);
        f4 acc[3];
#pragma unroll
        for (int i = 0; i < 2; ++i) {
            WR(8 * i, pkcn(b));
#pragma unroll
            for (int e = 1; e < 8; ++e) WR(8 * i + e, pkcn(cmul2(ze[e], b)));
            b = cmul2(b, z128);
        }
        acc[0] = (f4){0, 0, 0, 0};
#pragma unroll
        for (int k4 = 0; k4 < 8; ++k4) acc[0] = mfma_h(*(const u4*)(rp + 64 * k4), bfr[k4], acc[0]);
#pragma unroll
        for (int i = 0; i < 2; ++i) {
            WR(4 * i, pkcn(b));
#pragma unroll
            for (int e = 1; e < 4; ++e) WR(4 * i + e, pkcn(cmul2(ze[2 * e], b)));
            b = cmul2(b, z128);
        }
#pragma unroll
        for (int i = 0; i < 4; ++i) {
            WR(8 + 2 * i, pkcn(b));
            WR(8 + 2 * i + 1, pkcn(cmul2(b, ze[4])));
            if (i < 3) b = cmul2(b, z128);
        }
        acc[1] = (f4){0, 0, 0, 0};
#pragma unroll
        for (int k4 = 0; k4 < 8; ++k4) acc[1] = mfma_h(*(const u4*)(rp + 64 * k4), bfr[k4], acc[1]);
#pragma unroll
        for (int i = 0; i < 9; ++i) {
            WR(i, pkcn(bh));
            if (i < 8) bh = cmul2(bh, z128);
        }
        acc[2] = (f4){0, 0, 0, 0};
#pragma unroll
        for (int k4 = 0; k4 < 8; ++k4) acc[2] = mfma_h(*(const u4*)(rp + 64 * k4), bfr[k4], acc[2]);
        float* pd = (float*)sc;
        *(f4*)(pd + 128 * (g >> 1) + 4 * (g & 1) + 8 * r16) = acc[0] * S1inv;
        if (g < 2) *(f4*)(pd + 256 + 64 * g + 4 * r16) = acc[1] * S1inv;
        else {
            *(f2*)(pd + 384 + 64 * (g - 2) + 2 * r16) = (f2){acc[1][0], acc[1][1]} * S1inv;
            *(f2*)(pd + 384 + 64 * (g - 2) + 32 + 2 * r16) = (f2){acc[1][2], acc[1][3]} * S1inv;
        }
        if (g < 3) {
#pragma unroll
            for (int e = 0; e < 4; ++e) pd[512 + 16 * (4 * g + e) + r16] = acc[2][e] * S1inv;
        }
        if (lane == 0) *(float*)(lds + SSM_BD + 4 * wave) = bd;
    }
    TR(2);
    __syncthreads();
    float SKinv[4];
    {
        const float* bdp = (const float*)(lds + SSM_BD);
        float SKs[4];
#pragma unroll
        for (int c = 0; c < 4; ++c) {
            float bb = 2.f * (bdp[2 * c] + bdp[2 * c + 1]);
            int e0 = (int)((__float_as_uint(bb) >> 23) & 255u) - 126; e0 = e0 < -100 ? -100 : e0;
            SKinv[c] = __uint_as_float((unsigned)(127 - 15 + e0) << 23); SKs[c] = __uint_as_float((unsigned)(127 + 15 - e0) << 23);
        }
        const int c = tid >> 7, q = tid & 127;
        const float SK = c == 0 ? SKs[0] : c == 1 ? SKs[1] : c == 2 ? SKs[2] : SKs[3];
#pragma unroll
        for (int rep = 0; rep < 2; ++rep) {
            const int qq = q + 128 * rep;
            if (qq < 164) {
                const char* s0 = lds + SSM_KS + (2 * c) * KS_WAVE + qq * 16;
                f4 v = (*(const f4*)s0 + *(const f4*)(s0 + KS_WAVE)) * SK;
                *(u2*)(lds + SSM_KD + c * 1312 + qq * 8) = (u2){pk_f16(v[0], v[1]), pk_f16(v[2], v[3])};
            }
        }
    }
    __syncthreads();
    TR(3);
    {
        const int xa_i = tid & 255;
        int idx[8];
#pragma unroll
        for (int j = 0; j < 8; j += 2) {
            int l0 = 2047 - (8 * xa_i + j), l1 = l0 - 1;
            unsigned bits = pk_bf16((float)l0, (float)l1);
            idx[j] = l0 < 128 ? l0 : (int)((bits & 0xffffu) - 0x4280u);
            idx[j + 1] = l1 < 128 ? l1 : (int)((bits >> 16) - 0x4280u);
        }
#pragma unroll
        for (int rep = 0; rep < 2; ++rep) {
            const int c = (tid >> 8) + 2 * rep;
            const uint16_t* kd = (const uint16_t*)(lds + SSM_KD + c * 1312);
            u4 o;
#pragma unroll
            for (int j = 0; j < 4; ++j) o[j] = (unsigned)kd[idx[2 * j]] | ((unsigned)kd[idx[2 * j + 1]] << 16);
            char* kr = lds + SSM_KR + c * 4608;
            *(u4*)(kr + 16 * xa_i) = o;
            if (xa_i < 32) *(u4*)(kr + 4096 + 16 * xa_i) = (u4){0, 0, 0, 0};
        }
        if (tid < 128) { *(u2*)(lds + SSM_XC + (tid >> 5) * 16640 + ((tid >> 3) & 3) * 4160 + 8 * (tid & 7)) = (u2){0, 0}; }
    }
    if (wave == 7) wait_flags((const gu64*)(ws + WS_FX) + (sb & (NREP - 1)) * 64 + (lane & 31), true, tag);
    __syncthreads();
    TR(4);
    {
        const u4 st = __builtin_amdgcn_raw_buffer_load_b128(rs, WS_ST + tid * 16, 0, 16);
        float xv[4][4];
#pragma unroll
        for (int e = 0; e < 4; ++e) {
            const unsigned sw_ = st[e];
            const float mb = lo_f(sw_), rinv = __builtin_amdgcn_rcpf(hi_f(sw_));
#pragma unroll
            for (int h = 0; h < 2; ++h) {
                const unsigned uw = ucol[e][h];
                unsigned a = pk_bf16(lo_f(uw) - mb, hi_f(uw) - mb);
                unsigned r = pk_bf16(lo_f(a) * rinv, hi_f(a) * rinv);
                xv[2 * h][e] = lo_f(r); xv[2 * h + 1][e] = hi_f(r);
            }
        }
#pragma unroll
        for (int cc = 0; cc < 4; ++cc) {
            const unsigned d0 = pk_f16(xv[cc][0], xv[cc][1]), d1 = pk_f16(xv[cc][2], xv[cc][3]);
            const unsigned n0 = __shfl_down(d0, 1), n1 = __shfl_down(d1, 1);
            const unsigned mid = __builtin_amdgcn_alignbyte(d1, d0, 2), e1 = __builtin_amdgcn_alignbyte(n0, d1, 2), e2 = __builtin_amdgcn_alignbyte(n1, n0, 2);
            char* xc = lds + SSM_XC + cc * 16640 + 8 * (tid + 8);
            *(u2*)(xc) = (u2){d0, d1};
            if (lane < 63) {
                *(u2*)(xc + 4160) = (u2){mid, e1};
                *(u2*)(xc + 2 * 4160) = (u2){d1, n0};
                *(u2*)(xc + 3 * 4160) = (u2){e1, e2};
            } else {
                *(unsigned*)(xc + 4160) = mid; *(uint16_t*)(xc + 4160 + 4) = (uint16_t)(d1 >> 16);
                *(unsigned*)(xc + 2 * 4160) = d1;
                *(uint16_t*)(xc + 3 * 4160) = (uint16_t)(d1 >> 16);
            }
            if (lane == 0) {
                *(uint16_t*)(xc + 4160 - 2) = (uint16_t)d0;
                *(unsigned*)(xc + 2 * 4160 - 4) = d0;
                *(uint16_t*)(xc + 3 * 4160 - 6) = (uint16_t)d0; *(unsigned*)(xc + 3 * 4160 - 4) = mid;
            }
        }
    }
    __syncthreads();
    TR(5);
    f4 acc[8];
#pragma unroll
    for (int n = 0; n < 8; ++n) acc[n] = (f4){0, 0, 0, 0};
    {
        const int rh = wave & 1;
        const char* xc = lds + SSM_XC + ch * 16640 + ((r16 + 1) & 3) * 4160 + 8 * (10 - 2 * g + ((r16 + 1) >> 2)) + 64 * 4 * rh;
        const char* kr = lds + SSM_KR + ch * 4608 + 16 * (257 - 2 * r16 - g) + 64 * 4 * rh;
#pragma unroll
        for (int rr = 0; rr < 4; ++rr) {
            u4 af[8], bfv[8];
#pragma unroll
            for (int kp = 0; kp < 8; ++kp) {
                const char* p = xc + 64 * (8 * kp + rr);
                u2 a = *(const u2*)p, b = *(const u2*)(p + 8);
                af[kp] = (u4){a.x, a.y, b.x, b.y};
            }
#pragma unroll
            for (int j = 0; j < 8; ++j) bfv[j] = *(const u4*)(kr - 64 * (8 * j - rr));
#pragma unroll
            for (int n = 0; n < 8; ++n)
#pragma unroll
                for (int kp = 0; kp <= n; ++kp) acc[n] = mfma_h(af[kp], bfv[n - kp], acc[n]);
        }
    }
    TR(6);
    {
        char* red = lds + SSM_RED;
#pragma unroll
        for (int n = 0; n < 8; ++n) *(f4*)(red + ((wave * 8 + n) * 64 + lane) * 16) = acc[n];
    }
    __syncthreads();
    TR(7);
    {
        const char* red = lds + SSM_RED;
        const int t = 256 * wave + 16 * r16 + 4 * g;
        float s[4][4];
#pragma unroll
        for (int c = 0; c < 4; ++c) {
            f4 y = *(const f4*)(red + (((2 * c) * 8 + wave) * 64 + lane) * 16) + *(const f4*)(red + (((2 * c + 1) * 8 + wave) * 64 + lane) * 16);
            const u2 xx = *(const u2*)(lds + SSM_XC + c * 16640 + 2 * (t + 32));
            const float ds = bf2f(dsk[c0 + c]);
#pragma unroll
            for (int e = 0; e < 4; ++e) {
                const unsigned xw = xx[e >> 1];
                float x = (e & 1) ? hi_h(xw) : lo_h(xw);
                s[c][e] = rbf(y[e] * SKinv[c]) + rbf(x * ds);
            }
        }
        const unsigned soff = WS_S + sb * 16384 + t * 8;
        __builtin_amdgcn_raw_buffer_store_b128((u4){pk_bf16(s[0][0], s[1][0]), pk_bf16(s[2][0], s[3][0]), pk_bf16(s[0][1], s[1][1]), pk_bf16(s[2][1], s[3][1])}, rs, soff, 0, 16);
        __builtin_amdgcn_raw_buffer_store_b128((u4){pk_bf16(s[0][2], s[1][2]), pk_bf16(s[2][2], s[3][2]), pk_bf16(s[0][3], s[1][3]), pk_bf16(s[2][3], s[3][3])}, rs, soff + 16, 0, 16);
    }
    WAIT_VM(0);
    __syncthreads();
    if (tid >= 448 && tid < 448 + NREP) __hip_atomic_store((gu64*)(ws + WS_FS) + (tid - 448) * 128 + sb, tag, RLX_AGENT);
    TR(8);
    TRFLUSH;
}

__global__ __launch_bounds__(512, 2) void fused_kernel(const uint16_t* __restrict__ u, const uint16_t* __restrict__ lar, const uint16_t* __restrict__ aim,
                                                       const uint16_t* __restrict__ cr, const uint16_t* __restrict__ ci,
                                                       const uint16_t* __restrict__ ldt, const uint16_t* __restrict__ dsk,
                                                       const uint16_t* __restrict__ w1, const uint16_t* __restrict__ w2,
                                                       char* ws, uint16_t* __restrict__ out) {
    extern __shared__ __attribute__((aligned(16))) char lds[];
    const int b = blockIdx.x;
    if (b < NFFN) ffn_role(u, w1, w2, ws, out, lds, b);
    else ssm_role(u, lar, aim, cr, ci, ldt, dsk, ws, lds, b - NFFN);
}

extern "C" void kernel_launch(void* const* d_in, const int* in_sizes, int n_in,
                              void* d_out, int out_size, void* d_ws, size_t ws_size,
                              hipStream_t stream) {
    (void)hipFuncSetAttribute((const void*)fused_kernel, hipFuncAttributeMaxDynamicSharedMemorySize, LDS_BYTES);
    fused_kernel<<<NFFN + NSSM, 512, LDS_BYTES, stream>>>((const uint16_t*)d_in[0], (const uint16_t*)d_in[1], (const uint16_t*)d_in[2], (const uint16_t*)d_in[3],
                                                  (const uint16_t*)d_in[4], (const uint16_t*)d_in[5], (const uint16_t*)d_in[6], (const uint16_t*)d_in[8],
                                                  (const uint16_t*)d_in[9], (char*)d_ws, (uint16_t*)d_out);
}
```

```cpp
#include <hip/hip_runtime.h>
#include <stdint.h>

#define L_ 2048
#define D_ 448
#define N_ 256
#define F_ 1792

typedef __attribute__((ext_vector_type(8))) __bf16 bf8;
typedef __attribute__((ext_vector_type(8))) _Float16 h8;
typedef __attribute__((ext_vector_type(4))) short s4v;
typedef __attribute__((ext_vector_type(4))) float f4;
typedef __attribute__((ext_vector_type(2))) float f2;
typedef __attribute__((ext_vector_type(4))) unsigned u4;
typedef __attribute__((ext_vector_type(2))) unsigned u2;
typedef __attribute__((ext_vector_type(2))) __bf16 bf2v;
typedef __attribute__((ext_vector_type(2))) _Float16 h2v;
typedef unsigned long long u64;
#define LAS __attribute__((address_space(3)))
#define GAS __attribute__((address_space(1)))
typedef GAS u64 gu64;
#define RLX_AGENT __ATOMIC_RELAXED, __HIP_MEMORY_SCOPE_AGENT

__device__ __forceinline__ float bf2f(uint16_t v) { return __uint_as_float(((uint32_t)v) << 16); }
__device__ __forceinline__ unsigned pk_bf16(float a, float b) { bf2v r; r.x = (__bf16)a; r.y = (__bf16)b; return __builtin_bit_cast(unsigned, r); }
__device__ __forceinline__ unsigned pk_f16(float a, float b) { h2v r; r.x = (_Float16)a; r.y = (_Float16)b; return __builtin_bit_cast(unsigned, r); }
__device__ __forceinline__ float rbf(float f) { return __uint_as_float(pk_bf16(f, 0.f) << 16); }
__device__ __forceinline__ float lo_f(unsigned w) { return __uint_as_float(w << 16); }
__device__ __forceinline__ float hi_f(unsigned w) { return __uint_as_float(w & 0xffff0000u); }
__device__ __forceinline__ float lo_h(unsigned w) { return (float)__builtin_bit_cast(h2v, w).x; }
__device__ __forceinline__ float hi_h(unsigned w) { return (float)__builtin_bit_cast(h2v, w).y; }
__device__ __forceinline__ f4 mfma_bf(u4 a, u4 b, f4 c) { return __builtin_amdgcn_mfma_f32_16x16x32_bf16(__builtin_bit_cast(bf8, a), __builtin_bit_cast(bf8, b), c, 0, 0, 0); }
__device__ __forceinline__ f4 mfma_h(u4 a, u4 b, f4 c) { return __builtin_amdgcn_mfma_f32_16x16x32_f16(__builtin_bit_cast(h8, a), __builtin_bit_cast(h8, b), c, 0, 0, 0); }
template <int CTRL> __device__ __forceinline__ float dpp_f(float v) { return __int_as_float(__builtin_amdgcn_update_dpp(0, __float_as_int(v), CTRL, 0xf, 0xf, false)); }
__device__ __forceinline__ float row16_sum(float v) { v += dpp_f<0x128>(v); v += dpp_f<0x124>(v); v += dpp_f<0x122>(v); v += dpp_f<0x121>(v); return v; }
__device__ __forceinline__ float row16_max(float v) { v = fmaxf(v, dpp_f<0x128>(v)); v = fmaxf(v, dpp_f<0x124>(v)); v = fmaxf(v, dpp_f<0x122>(v)); v = fmaxf(v, dpp_f<0x121>(v)); return v; }
__device__ __forceinline__ u2 tr1(const char* p) { return __builtin_bit_cast(u2, __builtin_amdgcn_ds_read_tr16_b64_v4i16((LAS s4v*)p)); }
__device__ __forceinline__ void dma_pair(unsigned dst, unsigned va, unsigned vb, const void* sbase) {
    asm volatile("s_mov_b32 m0, %0\n\ts_nop 0\n\tglobal_load_lds_dwordx4 %1, %3\n\tglobal_load_lds_dwordx4 %2, %3 offset:1024" :: "s"(dst), "v"(va), "v"(vb), "s"(sbase) : "memory");
}
#define WAIT_VM(n) asm volatile("s_waitcnt vmcnt(" #n ")" ::: "memory")
#define WAIT_LGKM0() asm volatile("s_waitcnt lgkmcnt(0)" ::: "memory")
#define RAW_BAR() do { WAIT_LGKM0(); __builtin_amdgcn_s_barrier(); asm volatile("" ::: "memory"); } while (0)

#define SLEEPN 4

#define NFFN 128
#define NSSM 112
#define WS_EPOCH 0
#define NREP 16
#define WS_FX    65536
#define WS_ST    32768
#define WS_FS    131072
#define WS_FP    196608
#define WS_PSC   262144
#define WS_XT    (1u << 20)
#define WS_S     (3u << 20)
#define WS_PART  (5u << 20)
#define WS_BYTES (16u << 20)
#define LDS_BYTES 160000
#define SPIN_CAP (1u << 14)

__device__ __forceinline__ void wait_flags(const gu64* p, bool active, u64 tag) {
    for (unsigned spins = 0; spins < SPIN_CAP; ++spins) {
        u64 v = active ? __hip_atomic_load(p, RLX_AGENT) : tag;
        if (__all(v == tag)) break;
        __builtin_amdgcn_s_sleep(SLEEPN);
    }
    __builtin_amdgcn_fence(__ATOMIC_ACQUIRE, "wavefront");
}

#define XN_BYTES 57344
#define RING_OFF XN_BYTES
#define RSL 7
#define RING_W (RSL * 2048)
#define ST_OFF (RING_OFF + 7 * RING_W)
__device__ __forceinline__ void ffn_role(const uint16_t* __restrict__ u, const uint16_t* __restrict__ w1, const uint16_t* __restrict__ w2,
                                         char* ws, uint16_t* __restrict__ out, char* lds, int b) {
    const int tid = threadIdx.x, lane = tid & 63, wave = __builtin_amdgcn_readfirstlane(tid >> 6);
    const int r16 = lane & 15, g = lane >> 4;
    const int xcd = b & 7, ks = xcd & 3, rt = 16 * (xcd >> 2) + (b >> 3);
    const int rot = (b >> 3) % 14;
    const __amdgpu_buffer_rsrc_t rs = __builtin_amdgcn_make_buffer_rsrc(ws, 0, WS_BYTES, 0x00020000);
    u4 ur[2][4];
    {
        const u4* up = (const u4*)(u + (size_t)(64 * rt + 8 * wave + g) * D_) + (r16 < 14 ? r16 : 13);
        const u4* up2 = up + 4 * 56;
        asm volatile("global_load_dwordx4 %0, %8, off\n\tglobal_load_dwordx4 %1, %8, off offset:224\n\tglobal_load_dwordx4 %2, %8, off offset:448\n\tglobal_load_dwordx4 %3, %8, off offset:672\n\t"
                     "global_load_dwordx4 %4, %9, off\n\tglobal_load_dwordx4 %5, %9, off offset:224\n\tglobal_load_dwordx4 %6, %9, off offset:448\n\tglobal_load_dwordx4 %7, %9, off offset:672"
                     : "=&v"(ur[0][0]), "=&v"(ur[0][1]), "=&v"(ur[0][2]), "=&v"(ur[0][3]), "=&v"(ur[1][0]), "=&v"(ur[1][1]), "=&v"(ur[1][2]), "=&v"(ur[1][3])
                     : "v"(up), "v"(up2) : "memory");
    }
    const unsigned ldsbase = (unsigned)(size_t)(LAS char*)lds;
    const unsigned ring = ldsbase + RING_OFF + wave * RING_W;
    const int rho = lane >> 3;
    const unsigned srcc = (unsigned)(((lane & 7) ^ (((rho >> 1) & 3) << 1)) * 16);
    const int krow = 8 * (rho >> 2) + (rho & 3);
    const unsigned v1a = krow * (F_ * 2) + srcc, v1b = v1a + 16 * F_ * 2 - 1024;
    const unsigned v2a = krow * (D_ * 2) + srcc, v2b = v2a + 16 * D_ * 2 - 1024;
    const char* w1b = (const char*)(w1 + 448 * ks + 64 * wave);
    const char* w2b = (const char*)(w2 + (size_t)448 * ks * D_ + 64 * wave);
#define KROT(i) ((i) + rot >= 14 ? (i) + rot - 14 : (i) + rot)
#define ISSUE(s) do { if ((s) < 28) dma_pair(ring + ((s) % RSL) * 2048, v1a, v1b, w1b + (size_t)KROT((s) >> 1) * (32 * F_ * 2) + ((s) & 1) * (4 * F_ * 2)); \
                      else if ((s) < 56) dma_pair(ring + ((s) % RSL) * 2048, v2a, v2b, w2b + (size_t)KROT(((s) - 28) >> 1) * (32 * D_ * 2) + ((s) & 1) * (4 * D_ * 2)); } while (0)
    u64 tag = 0;
    if (wave < 7) { ISSUE(0); ISSUE(1); ISSUE(2); ISSUE(3); }
    else WAIT_VM(0);
    asm volatile("s_waitcnt vmcnt(8)" : "+v"(ur[0][0]), "+v"(ur[0][1]), "+v"(ur[0][2]), "+v"(ur[0][3]), "+v"(ur[1][0]), "+v"(ur[1][1]), "+v"(ur[1][2]), "+v"(ur[1][3]) :: "memory");
    if (wave == 7) { __builtin_amdgcn_s_setprio(3); tag = *(const u64*)(ws + WS_EPOCH) + 1ull; }
#pragma unroll
    for (int p = 0; p < 2; ++p) {
        const bool act = r16 < 14;
        const bf2v one2 = {(__bf16)1.0f, (__bf16)1.0f};
        float s = 0.f;
#pragma unroll
        for (int i = 0; i < 4; ++i)
#pragma unroll
            for (int e = 0; e < 4; ++e) { const unsigned w_ = ur[p][i][e]; s = __builtin_amdgcn_fdot2_f32_bf16(__builtin_bit_cast(bf2v, w_), one2, s, false); }
        s = row16_sum(act ? s : 0.f);
        const float mean = s * (1.0f / D_);
        const float mb = rbf(mean);
        unsigned av[4][4];
        f2 q2 = {0.f, 0.f};
#pragma unroll
        for (int i = 0; i < 4; ++i)
#pragma unroll
            for (int e = 0; e < 4; ++e) {
                f2 d = (f2){lo_f(ur[p][i][e]), hi_f(ur[p][i][e])} - (f2){mb, mb};
                q2 = __builtin_elementwise_fma(d, d, q2);
                av[i][e] = pk_bf16(d.x, d.y);
            }
#pragma unroll
        for (int i = 0; i < 4; ++i) asm volatile("" : "+v"(av[i][0]), "+v"(av[i][1]), "+v"(av[i][2]), "+v"(av[i][3]));
        const float q = row16_sum(act ? q2.x + q2.y : 0.f);
        const float dm = mean - mb;
        const float var = fmaxf(fmaf(-dm, dm, q * (1.0f / D_)), 0.f);
        const float sd = rbf(__builtin_amdgcn_sqrtf(rbf(rbf(var) + 1e-5f)));
        const float rinv = __builtin_amdgcn_rcpf(sd);
        const int lrow = 8 * wave + 4 * p + g;
        if (r16 == 0) *(unsigned*)(lds + ST_OFF + 4 * lrow) = (__float_as_uint(mb) >> 16) | (__float_as_uint(sd) & 0xffff0000u);
        char* xr = lds + lrow * 896;
        const int sw = (lrow >> 1) & 7;
#pragma unroll
        for (int i = 0; i < 4; ++i) {
            u4 o;
#pragma unroll
            for (int e = 0; e < 4; ++e) {
                f2 r = (f2){lo_f(av[i][e]), hi_f(av[i][e])} * (f2){rinv, rinv};
                o[e] = pk_bf16(r.x, r.y);
            }
            if (act) *(u4*)(xr + 16 * ((r16 + 14 * i) ^ sw)) = o;
        }
    }
    if (wave < 7) { ISSUE(4); ISSUE(5); ISSUE(6); }
    if (tid == 0) *(unsigned*)(lds + ST_OFF + 256) = 0u;
    RAW_BAR();
    const int q4 = r16 >> 2, p4 = lane & 3;
    if (wave == 7) {
        if (ks == 0) {
            __builtin_amdgcn_raw_buffer_store_b32(*(const unsigned*)(lds + ST_OFF + 4 * lane), rs, WS_ST + (64 * rt + lane) * 4, 0, 16);
            WAIT_VM(0);
            if (lane < NREP) __hip_atomic_store((gu64*)(ws + WS_FX) + lane * 64 + rt, tag, RLX_AGENT);
        }
        {
            const u4 ones = {0xffffffffu, 0xffffffffu, 0xffffffffu, 0xffffffffu};
            const unsigned pb = WS_PART + (unsigned)((rt * 4 + ks) * 56) * 1024 + lane * 16;
            for (int i = 0; i < 56; ++i) if (((i >> 1) & 3) != ks) __builtin_amdgcn_raw_buffer_store_b128(ones, rs, pb + i * 1024, 0, 16);
            __builtin_amdgcn_raw_buffer_store_b128(ones, rs, WS_PSC + (rt * 4 + ks) * 2048 + lane * 16, 0, 16);
            __builtin_amdgcn_raw_buffer_store_b128(ones, rs, WS_PSC + (rt * 4 + ks) * 2048 + 1024 + lane * 16, 0, 16);
            WAIT_VM(0);
            if (lane == 0) __hip_atomic_store((gu64*)(ws + WS_FP) + rt * 16 + ks, tag, RLX_AGENT);
        }
        __builtin_amdgcn_s_setprio(0);
        RAW_BAR();
        RAW_BAR();
        wait_flags((const gu64*)(ws + WS_FS) + (rt & (NREP - 1)) * 128 + lane, true, tag);
        {
            const gu64* p = lane < 48 ? (const gu64*)(ws + WS_FS) + (rt & (NREP - 1)) * 128 + 64 + lane : (const gu64*)(ws + WS_FP) + rt * 16 + (lane & 3);
            wait_flags(p, lane < 52, tag);
        }
        if (lane == 0) __hip_atomic_store((LAS unsigned*)((LAS char*)lds + ST_OFF + 256), 1u, __ATOMIC_RELAXED, __HIP_MEMORY_SCOPE_WORKGROUP);
        if (b == 0) {
#pragma unroll
            for (int i = 0; i < 2; ++i) wait_flags((const gu64*)(ws + WS_FP) + (16 * i + (lane >> 2)) * 16 + (lane & 3), true, tag);
            if (lane == 0) __hip_atomic_store((gu64*)(ws + WS_EPOCH), tag, RLX_AGENT);
        }
        return;
    }
    if (wave >= 4) __builtin_amdgcn_s_setprio(1);
    const int rr = 4 * g + q4;
    const int wtr = rr * 128 + (((rr >> 1) & 3) << 5) + 8 * p4;
    const char* rpb = lds + RING_OFF + wave * RING_W;
    const char* rp0 = rpb + wtr; const char* rp1 = rpb + (wtr ^ 32); const char* rp2 = rpb + (wtr ^ 64); const char* rp3 = rpb + (wtr ^ 96);
    const int asw = (r16 >> 1) & 7;
    const unsigned ae = (unsigned)((g ^ asw) * 16);
    const char* a0p = lds + r16 * 896;
    f4 acc[4][4];
#pragma unroll
    for (int m = 0; m < 4; ++m)
#pragma unroll
        for (int n = 0; n < 4; ++n) acc[m][n] = (f4){0, 0, 0, 0};
    u4 af[2][4]; u2 x[2][2][4];
#define VMW(n) do { if ((n) >= 10) WAIT_VM(10); else if ((n) >= 8) WAIT_VM(8); else if ((n) >= 6) WAIT_VM(6); else if ((n) >= 4) WAIT_VM(4); else if ((n) >= 2) WAIT_VM(2); else WAIT_VM(0); } while (0)
#define LOADW(j, bsel, issued) do { VMW(2 * ((issued) - (2 * (j) + 1))); \
        _Pragma("unroll") for (int hf = 0; hf < 2; ++hf) { const int so = ((2 * (j) + hf) % RSL) * 2048; \
            x[bsel][hf][0] = tr1(rp0 + so); x[bsel][hf][1] = tr1(rp1 + so); x[bsel][hf][2] = tr1(rp2 + so); x[bsel][hf][3] = tr1(rp3 + so); } } while (0)
#define LOADA(it, bsel) do { const char* ap = a0p + (ae ^ (unsigned)(KROT(it) << 6)); \
        _Pragma("unroll") for (int m = 0; m < 4; ++m) af[bsel][m] = *(const u4*)(ap + m * 16 * 896); } while (0)
#define REFILL(j) do { WAIT_LGKM0(); ISSUE(2 * (j) + RSL); ISSUE(2 * (j) + 1 + RSL); } while (0)
#define ISSUED(j) ((2 * (j) + 1 + RSL) < 55 ? (2 * (j) + 1 + RSL) : 55)
    LOADW(0, 0, RSL - 1); LOADA(0, 0);
    REFILL(0);
#pragma unroll
    for (int it = 0; it < 14; ++it) {
        const int cur = it & 1, nxt = cur ^ 1;
        LOADW(it + 1, nxt, ISSUED(it));
        if (it < 13) LOADA(it + 1, nxt);
        __builtin_amdgcn_sched_barrier(0);
#pragma unroll
        for (int n = 0; n < 4; ++n) {
            const u4 wf = {x[cur][0][n].x, x[cur][0][n].y, x[cur][1][n].x, x[cur][1][n].y};
#pragma unroll
            for (int m = 0; m < 4; ++m) acc[m][n] = mfma_bf(wf, af[cur][m], acc[m][n]);
            __builtin_amdgcn_sched_barrier(0);
            if (n == 1) { WAIT_LGKM0(); ISSUE(2 * (it + 1) + RSL); }
            if (n == 2) { ISSUE(2 * (it + 1) + 1 + RSL); }
            __builtin_amdgcn_sched_barrier(0);
        }
    }
    {
        u2 hp[4][4];
#pragma unroll
        for (int m = 0; m < 4; ++m)
#pragma unroll
            for (int n = 0; n < 4; ++n) {
                typedef __attribute__((ext_vector_type(2))) short s2v;
                const unsigned p0 = pk_bf16(acc[m][n][0], acc[m][n][1]), p1 = pk_bf16(acc[m][n][2], acc[m][n][3]);
                hp[m][n] = (u2){__builtin_bit_cast(unsigned, __builtin_elementwise_max(__builtin_bit_cast(s2v, p0), (s2v){0, 0})), __builtin_bit_cast(unsigned, __builtin_elementwise_max(__builtin_bit_cast(s2v, p1), (s2v){0, 0}))};
                acc[m][n] = (f4){0, 0, 0, 0};
            }
        __builtin_amdgcn_sched_barrier(0);
        RAW_BAR();
#pragma unroll
        for (int m = 0; m < 4; ++m)
#pragma unroll
            for (int n = 0; n < 4; ++n)
                *(u2*)(lds + (16 * m + r16) * 896 + 16 * ((8 * wave + 2 * n + (g >> 1)) ^ asw) + 8 * (g & 1)) = hp[m][n];
    }
    RAW_BAR();
    LOADA(0, 0);
#pragma unroll
    for (int it = 0; it < 14; ++it) {
        const int cur = it & 1, nxt = cur ^ 1;
        if (it < 13) { LOADW(14 + it + 1, nxt, ISSUED(14 + it)); LOADA(it + 1, nxt); }
        __builtin_amdgcn_sched_barrier(0);
#pragma unroll
        for (int n = 0; n < 4; ++n) {
            const u4 wf = {x[cur][0][n].x, x[cur][0][n].y, x[cur][1][n].x, x[cur][1][n].y};
#pragma unroll
            for (int m = 0; m < 4; ++m) acc[m][n] = mfma_bf(wf, af[cur][m], acc[m][n]);
            __builtin_amdgcn_sched_barrier(0);
            if (n == 1 && 2 * (14 + it + 1) + RSL < 56) { WAIT_LGKM0(); ISSUE(2 * (14 + it + 1) + RSL); }
            if (n == 2 && 2 * (14 + it + 1) + RSL < 56) { ISSUE(2 * (14 + it + 1) + 1 + RSL); }
            __builtin_amdgcn_sched_barrier(0);
        }
    }
    {
        float mx = 0.f;
#pragma unroll
        for (int m = 0; m < 4; ++m)
#pragma unroll
            for (int n = 0; n < 4; ++n)
#pragma unroll
                for (int e = 0; e < 4; ++e) mx = fmaxf(mx, fabsf(acc[m][n][e]));
        int e2 = (int)((__float_as_uint(mx) >> 23) & 255u) - 126;
        e2 = e2 < -100 ? -100 : e2;
        const float sc = __uint_as_float((unsigned)(127 + 15 - e2) << 23);
        const float scinv = __uint_as_float((unsigned)(127 - 15 + e2) << 23);
        const unsigned pbase = WS_PART + (unsigned)(((rt * 4 + ks) * 7 + wave) * 8) * 1024 + lane * 16;
#pragma unroll
        for (int m = 0; m < 4; ++m) {
            if (m != ks) {
#pragma unroll
                for (int np = 0; np < 2; ++np) {
                    u4 o = {pk_f16(acc[m][2 * np][0] * sc, acc[m][2 * np][1] * sc), pk_f16(acc[m][2 * np][2] * sc, acc[m][2 * np][3] * sc),
                            pk_f16(acc[m][2 * np + 1][0] * sc, acc[m][2 * np + 1][1] * sc), pk_f16(acc[m][2 * np + 1][2] * sc, acc[m][2 * np + 1][3] * sc)};
#pragma unroll
                    for (int e = 0; e < 4; ++e) o[e] = o[e] < 0xfffffffeu ? o[e] : 0xfffffffeu;
                    __builtin_amdgcn_raw_buffer_store_b128(o, rs, pbase + (2 * m + np) * 1024, 0, 16);
                }
            }
        }
        __builtin_amdgcn_raw_buffer_store_b32(__float_as_uint(scinv), rs, WS_PSC + (((rt * 4 + ks) * 8 + wave) * 64 + lane) * 4, 0, 16);
    }
    f4 own[4];
#pragma unroll
    for (int n = 0; n < 4; ++n) own[n] = ks == 0 ? acc[0][n] : ks == 1 ? acc[1][n] : ks == 2 ? acc[2][n] : acc[3][n];
    const int trow = 64 * rt + 16 * ks;
    const size_t uo = (size_t)(trow + (lane >> 3)) * D_ + 64 * wave + 8 * (lane & 7);
    const u4 uu0 = *(const u4*)(u + uo), uu1 = *(const u4*)(u + uo + 8 * D_);
    for (unsigned spins = 0; spins < (1u << 20); ++spins) {
        if (__hip_atomic_load((LAS unsigned*)((LAS char*)lds + ST_OFF + 256), __ATOMIC_RELAXED, __HIP_MEMORY_SCOPE_WORKGROUP)) break;
        __builtin_amdgcn_s_sleep(2);
    }
    asm volatile("" ::: "memory");
    {
        u2 sv[4];
#pragma unroll
        for (int n = 0; n < 4; ++n) sv[n] = __builtin_amdgcn_raw_buffer_load_b64(rs, WS_S + (16 * wave + 4 * n + g) * 16384 + (trow + r16) * 8, 0, 16);
        u4 pv[3][2]; float psc[3];
        for (unsigned spins = 0; spins < SPIN_CAP; ++spins) {
            unsigned mxv = 0;
#pragma unroll
            for (int j = 0; j < 3; ++j) {
                const int kp = (ks + 1 + j) & 3;
#pragma unroll
                for (int np = 0; np < 2; ++np)
                    pv[j][np] = __builtin_amdgcn_raw_buffer_load_b128(rs, WS_PART + (unsigned)((((rt * 4 + kp) * 7 + wave) * 8) + 2 * ks + np) * 1024 + lane * 16, 0, 16);
                psc[j] = __uint_as_float(__builtin_amdgcn_raw_buffer_load_b32(rs, WS_PSC + (((rt * 4 + kp) * 8 + wave) * 64 + lane) * 4, 0, 16));
            }
#pragma unroll
            for (int j = 0; j < 3; ++j) {
#pragma unroll
                for (int np = 0; np < 2; ++np)
#pragma unroll
                    for (int e = 0; e < 4; ++e) { const unsigned w_ = pv[j][np][e]; mxv = mxv > w_ ? mxv : w_; }
                const unsigned w_ = __float_as_uint(psc[j]); mxv = mxv > w_ ? mxv : w_;
            }
            if (!__any(mxv == 0xffffffffu)) break;
            __builtin_amdgcn_s_sleep(1);
            asm volatile("" ::: "memory");
        }
#pragma unroll
        for (int j = 0; j < 3; ++j)
#pragma unroll
            for (int n = 0; n < 4; ++n) {
                const unsigned w0 = pv[j][n >> 1][2 * (n & 1)], w1_ = pv[j][n >> 1][2 * (n & 1) + 1];
                own[n][0] = fmaf(lo_h(w0), psc[j], own[n][0]); own[n][1] = fmaf(hi_h(w0), psc[j], own[n][1]);
                own[n][2] = fmaf(lo_h(w1_), psc[j], own[n][2]); own[n][3] = fmaf(hi_h(w1_), psc[j], own[n][3]);
            }
        char* tb = lds + RING_OFF + wave * RING_W;
#pragma unroll
        for (int n = 0; n < 4; ++n) {
            float sg[4];
#pragma unroll
            for (int e = 0; e < 4; ++e) {
                const unsigned sw_ = sv[n][e >> 1];
                float s = (e & 1) ? hi_f(sw_) : lo_f(sw_);
                float o = rbf(own[n][e]);
                float gg = rbf(o * __builtin_amdgcn_rcpf(1.f + __expf(-o)));
                sg[e] = s * gg;
            }
            *(u2*)(tb + r16 * 144 + 32 * n + 8 * g) = (u2){pk_bf16(sg[0], sg[1]), pk_bf16(sg[2], sg[3])};
        }
#pragma unroll
        for (int h = 0; h < 2; ++h) {
            const u4 sgv = *(const u4*)(tb + (8 * h + (lane >> 3)) * 144 + 16 * (lane & 7));
            const u4 uv = h ? uu1 : uu0;
            u4 o;
#pragma unroll
            for (int e = 0; e < 4; ++e) { const unsigned a = uv[e], c = sgv[e]; o[e] = pk_bf16(lo_f(a) + lo_f(c), hi_f(a) + hi_f(c)); }
            *(u4*)(out + uo + (size_t)h * 8 * D_) = o;
        }
    }
}

__device__ __forceinline__ f2 cmul(f2 a, f2 b) {
    f2 t = a * b.xx;
    f2 by = b.yy; by.x = -by.x;
    return __builtin_elementwise_fma(a.yx, by, t);
}
__device__ __forceinline__ f2 zpow(float a2, float wv, float p) {
    float e = __builtin_amdgcn_exp2f(a2 * p);
    float th = __builtin_amdgcn_fractf(wv * p);
    return (f2){e * __builtin_amdgcn_cosf(th), e * __builtin_amdgcn_sinf(th)};
}
struct c2 { f2 a, b; };
__device__ __forceinline__ c2 cmul2(c2 x, c2 y) { c2 o; o.a = cmul(x.a, y.a); o.b = cmul(x.b, y.b); return o; }
__device__ __forceinline__ u2 pkc(c2 v) { return (u2){pk_f16(v.a.x, v.a.y), pk_f16(v.b.x, v.b.y)}; }
__device__ __forceinline__ u2 pkcn(c2 v) { return (u2){pk_f16(v.a.x, -v.a.y), pk_f16(v.b.x, -v.b.y)}; }
#define KS_ROW 528
#define KS_WAVE (16 * KS_ROW)
#define SSM_KS   0
#define SSM_XC   67584
#define SSM_KR   134144
#define SSM_KD   152576
#define SSM_BD   157824
#define SSM_RED  0

__device__ __forceinline__ void ssm_role(const uint16_t* __restrict__ u, const uint16_t* __restrict__ lar, const uint16_t* __restrict__ aim,
                                         const uint16_t* __restrict__ cr, const uint16_t* __restrict__ ci,
                                         const uint16_t* __restrict__ ldt, const uint16_t* __restrict__ dsk,
                                         char* ws, char* lds, int sb) {
    const int tid = threadIdx.x, lane = tid & 63, wave = __builtin_amdgcn_readfirstlane(tid >> 6);
    const int r16 = lane & 15, g = lane >> 4;
    const int c0 = 4 * sb;
    const int ch = wave >> 1;
    u64 tag = 0;
    const __amdgpu_buffer_rsrc_t rs = __builtin_amdgcn_make_buffer_rsrc(ws, 0, WS_BYTES, 0x00020000);
    u2 ucol[4];
    {
        const int c = c0 + ch, n0 = c * N_ + 128 * (wave & 1) + 2 * lane;
        const unsigned wl = *(const unsigned*)(lar + n0), wa = *(const unsigned*)(aim + n0), wr = *(const unsigned*)(cr + n0), wi = *(const unsigned*)(ci + n0);
        const uint16_t wdt = ldt[c];
        if (wave == 7) tag = *(const u64*)(ws + WS_EPOCH) + 1ull;
        const float dt = rbf(expf(bf2f(wdt)));
        const float ar0 = -rbf(expf(lo_f(wl))) * dt, ar1 = -rbf(expf(hi_f(wl))) * dt;
        const float ai0 = lo_f(wa) * dt, ai1 = hi_f(wa) * dt;
        const float Cr0 = lo_f(wr) * dt, Cr1 = hi_f(wr) * dt, Ci0 = lo_f(wi) * dt, Ci1 = hi_f(wi) * dt;
        float bd = row16_sum((fabsf(Cr0) + fabsf(Ci0)) + (fabsf(Cr1) + fabsf(Ci1)));
        bd += __shfl_xor(bd, 16); bd += __shfl_xor(bd, 32);
        int eb = (int)((__float_as_uint(bd) >> 23) & 255u) - 126;
        eb = eb < -100 ? -100 : eb;
        const float S1 = __uint_as_float((unsigned)(127 + 14 - eb) << 23);
        const float S1inv = __uint_as_float((unsigned)(127 + eb - 27) << 23);
        const float a20 = ar0 * 1.44269504088896f, wv0 = ai0 * 0.159154943091895f, a21 = ar1 * 1.44269504088896f, wv1 = ai1 * 0.159154943091895f;
        char* sc = lds + SSM_KS + wave * KS_WAVE;
        char* wp = sc + 8 * lane;
        const char* rp = sc + r16 * KS_ROW + 16 * g;
#define ZP(p) (c2){zpow(a20, wv0, p), zpow(a21, wv1, p)}
        c2 z1 = ZP(1.f), z8 = ZP(8.f), z64 = ZP(64.f), z128 = ZP(128.f), z1024 = ZP(1024.f);
#define WR(e, v) *(u2*)(wp + (e) * KS_ROW) = (v)
        u4 bfr[8];
        {
            c2 cj = {(f2){16384.f, 0.f}, (f2){16384.f, 0.f}};
#pragma unroll
            for (int j = 0; j < 8; ++j) {
                c2 hi = cmul2(cj, z64);
                WR(j, pkc(cj)); WR(8 + j, pkc(hi));
                if (j < 7) cj = cmul2(cj, z8);
            }
#pragma unroll
            for (int k4 = 0; k4 < 8; ++k4) bfr[k4] = *(const u4*)(rp + 64 * k4);
        }
        asm volatile("" :: "v"(bfr[0]) : "memory");
#pragma unroll
        for (int e = 0; e < 4; ++e) ucol[e] = *(const u2*)(u + (size_t)(4 * tid + e) * D_ + c0);
        c2 ze[8];
        ze[1] = z1; ze[2] = cmul2(z1, z1); ze[3] = cmul2(ze[2], z1); ze[4] = cmul2(ze[2], ze[2]);
        ze[5] = cmul2(ze[4], z1); ze[6] = cmul2(ze[4], ze[2]); ze[7] = cmul2(ze[4], ze[3]);
        c2 b = {(f2){Cr0 * S1, Ci0 * S1}, (f2){Cr1 * S1, Ci1 * S1}};
        c2 bh = cmul2(b, z1024);
        f4 acc[3];
#pragma unroll
        for (int i = 0; i < 2; ++i) {
            WR(8 * i, pkcn(b));
#pragma unroll
            for (int e = 1; e < 8; ++e) WR(8 * i + e, pkcn(cmul2(ze[e], b)));
            b = cmul2(b, z128);
        }
        acc[0] = (f4){0, 0, 0, 0};
#pragma unroll
        for (int k4 = 0; k4 < 8; ++k4) acc[0] = mfma_h(*(const u4*)(rp + 64 * k4), bfr[k4], acc[0]);
#pragma unroll
        for (int i = 0; i < 2; ++i) {
            WR(4 * i, pkcn(b));
#pragma unroll
            for (int e = 1; e < 4; ++e) WR(4 * i + e, pkcn(cmul2(ze[2 * e], b)));
            b = cmul2(b, z128);
        }
#pragma unroll
        for (int i = 0; i < 4; ++i) {
            WR(8 + 2 * i, pkcn(b));
            WR(8 + 2 * i + 1, pkcn(cmul2(b, ze[4])));
            if (i < 3) b = cmul2(b, z128);
        }
        acc[1] = (f4){0, 0, 0, 0};
#pragma unroll
        for (int k4 = 0; k4 < 8; ++k4) acc[1] = mfma_h(*(const u4*)(rp + 64 * k4), bfr[k4], acc[1]);
#pragma unroll
        for (int i = 0; i < 9; ++i) {
            WR(i, pkcn(bh));
            if (i < 8) bh = cmul2(bh, z128);
        }
        acc[2] = (f4){0, 0, 0, 0};
#pragma unroll
        for (int k4 = 0; k4 < 8; ++k4) acc[2] = mfma_h(*(const u4*)(rp + 64 * k4), bfr[k4], acc[2]);
        float* pd = (float*)sc;
        *(f4*)(pd + 128 * (g >> 1) + 4 * (g & 1) + 8 * r16) = acc[0] * S1inv;
        if (g < 2) *(f4*)(pd + 256 + 64 * g + 4 * r16) = acc[1] * S1inv;
        else {
            *(f2*)(pd + 384 + 64 * (g - 2) + 2 * r16) = (f2){acc[1][0], acc[1][1]} * S1inv;
            *(f2*)(pd + 384 + 64 * (g - 2) + 32 + 2 * r16) = (f2){acc[1][2], acc[1][3]} * S1inv;
        }
        if (g < 3) {
#pragma unroll
            for (int e = 0; e < 4; ++e) pd[512 + 16 * (4 * g + e) + r16] = acc[2][e] * S1inv;
        }
        if (lane == 0) *(float*)(lds + SSM_BD + 4 * wave) = bd;
    }
    __syncthreads();
    float SKinv[4];
    {
        const float* bdp = (const float*)(lds + SSM_BD);
        float SKs[4];
#pragma unroll
        for (int c = 0; c < 4; ++c) {
            float bb = 2.f * (bdp[2 * c] + bdp[2 * c + 1]);
            int e0 = (int)((__float_as_uint(bb) >> 23) & 255u) - 126; e0 = e0 < -100 ? -100 : e0;
            SKinv[c] = __uint_as_float((unsigned)(127 - 15 + e0) << 23); SKs[c] = __uint_as_float((unsigned)(127 + 15 - e0) << 23);
        }
        const int c = tid >> 7, q = tid & 127;
        const float SK = c == 0 ? SKs[0] : c == 1 ? SKs[1] : c == 2 ? SKs[2] : SKs[3];
#pragma unroll
        for (int rep = 0; rep < 2; ++rep) {
            const int qq = q + 128 * rep;
            if (qq < 164) {
                const char* s0 = lds + SSM_KS + (2 * c) * KS_WAVE + qq * 16;
                f4 v = (*(const f4*)s0 + *(const f4*)(s0 + KS_WAVE)) * SK;
                *(u2*)(lds + SSM_KD + c * 1312 + qq * 8) = (u2){pk_f16(v[0], v[1]), pk_f16(v[2], v[3])};
            }
        }
    }
    __syncthreads();
    {
        const int xa_i = tid & 255;
        int idx[8];
#pragma unroll
        for (int j = 0; j < 8; j += 2) {
            int l0 = 2047 - (8 * xa_i + j), l1 = l0 - 1;
            unsigned bits = pk_bf16((float)l0, (float)l1);
            idx[j] = l0 < 128 ? l0 : (int)((bits & 0xffffu) - 0x4280u);
            idx[j + 1] = l1 < 128 ? l1 : (int)((bits >> 16) - 0x4280u);
        }
#pragma unroll
        for (int rep = 0; rep < 2; ++rep) {
            const int c = (tid >> 8) + 2 * rep;
            const uint16_t* kd = (const uint16_t*)(lds + SSM_KD + c * 1312);
            u4 o;
#pragma unroll
            for (int j = 0; j < 4; ++j) o[j] = (unsigned)kd[idx[2 * j]] | ((unsigned)kd[idx[2 * j + 1]] << 16);
            char* kr = lds + SSM_KR + c * 4608;
            *(u4*)(kr + 16 * xa_i) = o;
            if (xa_i < 32) *(u4*)(kr + 4096 + 16 * xa_i) = (u4){0, 0, 0, 0};
        }
        if (tid < 128) { *(u2*)(lds + SSM_XC + (tid >> 5) * 16640 + ((tid >> 3) & 3) * 4160 + 8 * (tid & 7)) = (u2){0, 0}; }
    }
    if (wave == 7) wait_flags((const gu64*)(ws + WS_FX) + (sb & (NREP - 1)) * 64 + (lane & 31), true, tag);
    __syncthreads();
    {
        const u4 st = __builtin_amdgcn_raw_buffer_load_b128(rs, WS_ST + tid * 16, 0, 16);
        float xv[4][4];
#pragma unroll
        for (int e = 0; e < 4; ++e) {
            const unsigned sw_ = st[e];
            const float mb = lo_f(sw_), rinv = __builtin_amdgcn_rcpf(hi_f(sw_));
#pragma unroll
            for (int h = 0; h < 2; ++h) {
                const unsigned uw = ucol[e][h];
                unsigned a = pk_bf16(lo_f(uw) - mb, hi_f(uw) - mb);
                unsigned r = pk_bf16(lo_f(a) * rinv, hi_f(a) * rinv);
                xv[2 * h][e] = lo_f(r); xv[2 * h + 1][e] = hi_f(r);
            }
        }
#pragma unroll
        for (int cc = 0; cc < 4; ++cc) {
            const unsigned d0 = pk_f16(xv[cc][0], xv[cc][1]), d1 = pk_f16(xv[cc][2], xv[cc][3]);
            const unsigned n0 = __shfl_down(d0, 1), n1 = __shfl_down(d1, 1);
            const unsigned mid = __builtin_amdgcn_alignbyte(d1, d0, 2), e1 = __builtin_amdgcn_alignbyte(n0, d1, 2), e2 = __builtin_amdgcn_alignbyte(n1, n0, 2);
            char* xc = lds + SSM_XC + cc * 16640 + 8 * (tid + 8);
            *(u2*)(xc) = (u2){d0, d1};
            if (lane < 63) {
                *(u2*)(xc + 4160) = (u2){mid, e1};
                *(u2*)(xc + 2 * 4160) = (u2){d1, n0};
                *(u2*)(xc + 3 * 4160) = (u2){e1, e2};
            } else {
                *(unsigned*)(xc + 4160) = mid; *(uint16_t*)(xc + 4160 + 4) = (uint16_t)(d1 >> 16);
                *(unsigned*)(xc + 2 * 4160) = d1;
                *(uint16_t*)(xc + 3 * 4160) = (uint16_t)(d1 >> 16);
            }
            if (lane == 0) {
                *(uint16_t*)(xc + 4160 - 2) = (uint16_t)d0;
                *(unsigned*)(xc + 2 * 4160 - 4) = d0;
                *(uint16_t*)(xc + 3 * 4160 - 6) = (uint16_t)d0; *(unsigned*)(xc + 3 * 4160 - 4) = mid;
            }
        }
    }
    __syncthreads();
    f4 acc[8];
#pragma unroll
    for (int n = 0; n < 8; ++n) acc[n] = (f4){0, 0, 0, 0};
    {
        const int rh = wave & 1;
        const char* xc = lds + SSM_XC + ch * 16640 + ((r16 + 1) & 3) * 4160 + 8 * (10 - 2 * g + ((r16 + 1) >> 2)) + 64 * 4 * rh;
        const char* kr = lds + SSM_KR + ch * 4608 + 16 * (257 - 2 * r16 - g) + 64 * 4 * rh;
#pragma unroll
        for (int rr = 0; rr < 4; ++rr) {
            u4 af[8], bfv[8];
#pragma unroll
            for (int kp = 0; kp < 8; ++kp) {
                const char* p = xc + 64 * (8 * kp + rr);
                u2 a = *(const u2*)p, b = *(const u2*)(p + 8);
                af[kp] = (u4){a.x, a.y, b.x, b.y};
            }
#pragma unroll
            for (int j = 0; j < 8; ++j) bfv[j] = *(const u4*)(kr - 64 * (8 * j - rr));
#pragma unroll
            for (int n = 0; n < 8; ++n)
#pragma unroll
                for (int kp = 0; kp <= n; ++kp) acc[n] = mfma_h(af[kp], bfv[n - kp], acc[n]);
        }
    }
    {
        char* red = lds + SSM_RED;
#pragma unroll
        for (int n = 0; n < 8; ++n) *(f4*)(red + ((wave * 8 + n) * 64 + lane) * 16) = acc[n];
    }
    __syncthreads();
    {
        const char* red = lds + SSM_RED;
        const int t = 256 * wave + 16 * r16 + 4 * g;
        float s[4][4];
#pragma unroll
        for (int c = 0; c < 4; ++c) {
            f4 y = *(const f4*)(red + (((2 * c) * 8 + wave) * 64 + lane) * 16) + *(const f4*)(red + (((2 * c + 1) * 8 + wave) * 64 + lane) * 16);
            const u2 xx = *(const u2*)(lds + SSM_XC + c * 16640 + 2 * (t + 32));
            const float ds = bf2f(dsk[c0 + c]);
#pragma unroll
            for (int e = 0; e < 4; ++e) {
                const unsigned xw = xx[e >> 1];
                float x = (e & 1) ? hi_h(xw) : lo_h(xw);
                s[c][e] = rbf(y[e] * SKinv[c]) + rbf(x * ds);
            }
        }
        const unsigned soff = WS_S + sb * 16384 + t * 8;
        __builtin_amdgcn_raw_buffer_store_b128((u4){pk_bf16(s[0][0], s[1][0]), pk_bf16(s[2][0], s[3][0]), pk_bf16(s[0][1], s[1][1]), pk_bf16(s[2][1], s[3][1])}, rs, soff, 0, 16);
        __builtin_amdgcn_raw_buffer_store_b128((u4){pk_bf16(s[0][2], s[1][2]), pk_bf16(s[2][2], s[3][2]), pk_bf16(s[0][3], s[1][3]), pk_bf16(s[2][3], s[3][3])}, rs, soff + 16, 0, 16);
    }
    WAIT_VM(0);
    __syncthreads();
    if (tid >= 448 && tid < 448 + NREP) __hip_atomic_store((gu64*)(ws + WS_FS) + (tid - 448) * 128 + sb, tag, RLX_AGENT);
}

__global__ __launch_bounds__(512, 2) void fused_kernel(const uint16_t* __restrict__ u, const uint16_t* __restrict__ lar, const uint16_t* __restrict__ aim,
                                                       const uint16_t* __restrict__ cr, const uint16_t* __restrict__ ci,
                                                       const uint16_t* __restrict__ ldt, const uint16_t* __restrict__ dsk,
                                                       const uint16_t* __restrict__ w1, const uint16_t* __restrict__ w2,
                                                       char* ws, uint16_t* __restrict__ out) {
    extern __shared__ __attribute__((aligned(16))) char lds[];
    const int b = blockIdx.x;
    if (b < NFFN) ffn_role(u, w1, w2, ws, out, lds, b);
    else ssm_role(u, lar, aim, cr, ci, ldt, dsk, ws, lds, b - NFFN);
}

extern "C" void kernel_launch(void* const* d_in, const int* in_sizes, int n_in,
                              void* d_out, int out_size, void* d_ws, size_t ws_size,
                              hipStream_t stream) {
    (void)hipFuncSetAttribute((const void*)fused_kernel, hipFuncAttributeMaxDynamicSharedMemorySize, LDS_BYTES);
    fused_kernel<<<NFFN + NSSM, 512, LDS_BYTES, stream>>>((const uint16_t*)d_in[0], (const uint16_t*)d_in[1], (const uint16_t*)d_in[2], (const uint16_t*)d_in[3],
                                                  (const uint16_t*)d_in[4], (const uint16_t*)d_in[5], (const uint16_t*)d_in[6], (const uint16_t*)d_in[8],
                                                  (const uint16_t*)d_in[9], (char*)d_ws, (uint16_t*)d_out);
}
```

```cpp
#include <hip/hip_runtime.h>
#include <stdint.h>

#define L_ 2048
#define D_ 448
#define N_ 256
#define F_ 1792

typedef __attribute__((ext_vector_type(8))) __bf16 bf8;
typedef __attribute__((ext_vector_type(8))) _Float16 h8;
typedef __attribute__((ext_vector_type(4))) short s4v;
typedef __attribute__((ext_vector_type(4))) float f4;
typedef __attribute__((ext_vector_type(2))) float f2;
typedef __attribute__((ext_vector_type(4))) unsigned u4;
typedef __attribute__((ext_vector_type(2))) unsigned u2;
typedef __attribute__((ext_vector_type(2))) __bf16 bf2v;
typedef __attribute__((ext_vector_type(2))) _Float16 h2v;
typedef unsigned long long u64;
#define LAS __attribute__((address_space(3)))
#define GAS __attribute__((address_space(1)))
typedef GAS u64 gu64;
#define RLX_AGENT __ATOMIC_RELAXED, __HIP_MEMORY_SCOPE_AGENT

__device__ __forceinline__ float bf2f(uint16_t v) { return __uint_as_float(((uint32_t)v) << 16); }
__device__ __forceinline__ unsigned pk_bf16(float a, float b) { bf2v r; r.x = (__bf16)a; r.y = (__bf16)b; return __builtin_bit_cast(unsigned, r); }
__device__ __forceinline__ unsigned pk_f16(float a, float b) { h2v r; r.x = (_Float16)a; r.y = (_Float16)b; return __builtin_bit_cast(unsigned, r); }
__device__ __forceinline__ float rbf(float f) { return __uint_as_float(pk_bf16(f, 0.f) << 16); }
__device__ __forceinline__ float lo_f(unsigned w) { return __uint_as_float(w << 16); }
__device__ __forceinline__ float hi_f(unsigned w) { return __uint_as_float(w & 0xffff0000u); }
__device__ __forceinline__ float lo_h(unsigned w) { return (float)__builtin_bit_cast(h2v, w).x; }
__device__ __forceinline__ float hi_h(unsigned w) { return (float)__builtin_bit_cast(h2v, w).y; }
__device__ __forceinline__ f4 mfma_bf(u4 a, u4 b, f4 c) { return __builtin_amdgcn_mfma_f32_16x16x32_bf16(__builtin_bit_cast(bf8, a), __builtin_bit_cast(bf8, b), c, 0, 0, 0); }
__device__ __forceinline__ f4 mfma_h(u4 a, u4 b, f4 c) { return __builtin_amdgcn_mfma_f32_16x16x32_f16(__builtin_bit_cast(h8, a), __builtin_bit_cast(h8, b), c, 0, 0, 0); }
template <int CTRL> __device__ __forceinline__ float dpp_f(float v) { return __int_as_float(__builtin_amdgcn_update_dpp(0, __float_as_int(v), CTRL, 0xf, 0xf, false)); }
__device__ __forceinline__ float row16_sum(float v) { v += dpp_f<0x128>(v); v += dpp_f<0x124>(v); v += dpp_f<0x122>(v); v += dpp_f<0x121>(v); return v; }
__device__ __forceinline__ float row16_max(float v) { v = fmaxf(v, dpp_f<0x128>(v)); v = fmaxf(v, dpp_f<0x124>(v)); v = fmaxf(v, dpp_f<0x122>(v)); v = fmaxf(v, dpp_f<0x121>(v)); return v; }
__device__ __forceinline__ u2 tr1(const char* p) { return __builtin_bit_cast(u2, __builtin_amdgcn_ds_read_tr16_b64_v4i16((LAS s4v*)p)); }
__device__ __forceinline__ void dma_pair(unsigned dst, unsigned va, unsigned vb, const void* sbase) {
    asm volatile("s_mov_b32 m0, %0\n\ts_nop 0\n\tglobal_load_lds_dwordx4 %1, %3\n\tglobal_load_lds_dwordx4 %2, %3 offset:1024" :: "s"(dst), "v"(va), "v"(vb), "s"(sbase) : "memory");
}
typedef __attribute__((ext_vector_type(4))) int i4;
__device__ __forceinline__ void dma4(unsigned dst, unsigned v0, unsigned v1, unsigned v2, unsigned v3, i4 rsrc, unsigned soff) {
    asm volatile("s_mov_b32 m0, %0\n\ts_nop 0\n\tbuffer_load_dwordx4 %1, %5, %6 offen lds\n\tbuffer_load_dwordx4 %2, %5, %6 offen offset:1024 lds\n\t"
                 "buffer_load_dwordx4 %3, %5, %6 offen offset:2048 lds\n\tbuffer_load_dwordx4 %4, %5, %6 offen offset:3072 lds"
                 :: "s"(dst), "v"(v0), "v"(v1), "v"(v2), "v"(v3), "s"(rsrc), "s"(soff) : "memory");
}
__device__ __forceinline__ i4 mk_rsrc(const void* p, unsigned bytes) { const u64 a = (u64)p; return (i4){(int)(unsigned)a, (int)((unsigned)(a >> 32) & 0xffffu), (int)bytes, 0x00020000}; }
#define WAIT_VM(n) asm volatile("s_waitcnt vmcnt(" #n ")" ::: "memory")
#define WAIT_LGKM0() asm volatile("s_waitcnt lgkmcnt(0)" ::: "memory")
#define RAW_BAR() do { WAIT_LGKM0(); __builtin_amdgcn_s_barrier(); asm volatile("" ::: "memory"); } while (0)

#define SLEEPN 4

#define NFFN 128
#define NSSM 112
#define WS_EPOCH 0
#define NREP 16
#define WS_FX    65536
#define WS_ST    32768
#define WS_FS    131072
#define WS_FP    196608
#define WS_PSC   262144
#define WS_S     (3u << 20)
#define WS_PART  (5u << 20)
#define WS_BYTES (16u << 20)
#define LDS_BYTES 160000
#define SPIN_CAP (1u << 14)

__device__ __forceinline__ void wait_flags(const gu64* p, bool active, u64 tag) {
    for (unsigned spins = 0; spins < SPIN_CAP; ++spins) {
        u64 v = active ? __hip_atomic_load(p, RLX_AGENT) : tag;
        if (__all(v == tag)) break;
        __builtin_amdgcn_s_sleep(SLEEPN);
    }
    __builtin_amdgcn_fence(__ATOMIC_ACQUIRE, "wavefront");
}

#define XN_BYTES 57344
#define RING_OFF XN_BYTES
#define RSL 3
#define RING_W (RSL * 4096)
#define ST_OFF (RING_OFF + 7 * RING_W)
__device__ __forceinline__ void ffn_role(const uint16_t* __restrict__ u, const uint16_t* __restrict__ w1, const uint16_t* __restrict__ w2,
                                         char* ws, uint16_t* __restrict__ out, char* lds, int b) {
    const int tid = threadIdx.x, lane = tid & 63, wave = __builtin_amdgcn_readfirstlane(tid >> 6);
    const int r16 = lane & 15, g = lane >> 4;
    const int xcd = b & 7, ks = xcd & 3, rt = 16 * (xcd >> 2) + (b >> 3);
    const int rot = (b >> 3) % 14;
    const __amdgpu_buffer_rsrc_t rs = __builtin_amdgcn_make_buffer_rsrc(ws, 0, WS_BYTES, 0x00020000);
    u4 ur[2][4];
    {
        const u4* up = (const u4*)(u + (size_t)(64 * rt + 8 * wave + g) * D_) + (r16 < 14 ? r16 : 13);
        const u4* up2 = up + 4 * 56;
        asm volatile("global_load_dwordx4 %0, %8, off\n\tglobal_load_dwordx4 %1, %8, off offset:224\n\tglobal_load_dwordx4 %2, %8, off offset:448\n\tglobal_load_dwordx4 %3, %8, off offset:672\n\t"
                     "global_load_dwordx4 %4, %9, off\n\tglobal_load_dwordx4 %5, %9, off offset:224\n\tglobal_load_dwordx4 %6, %9, off offset:448\n\tglobal_load_dwordx4 %7, %9, off offset:672"
                     : "=&v"(ur[0][0]), "=&v"(ur[0][1]), "=&v"(ur[0][2]), "=&v"(ur[0][3]), "=&v"(ur[1][0]), "=&v"(ur[1][1]), "=&v"(ur[1][2]), "=&v"(ur[1][3])
                     : "v"(up), "v"(up2) : "memory");
    }
    const unsigned ldsbase = (unsigned)(size_t)(LAS char*)lds;
    const unsigned ring = ldsbase + RING_OFF + wave * RING_W;
    const int rho = lane >> 3;
    const unsigned srcc = (unsigned)(((lane & 7) ^ (((rho >> 1) & 3) << 1)) * 16);
    const int krow = 8 * (rho >> 2) + (rho & 3);
    const unsigned c1 = (unsigned)(448 * ks + 64 * wave) * 2u + srcc, c2 = (unsigned)(448 * ks * D_ + 64 * wave) * 2u + srcc;
    const unsigned v10 = krow * (F_ * 2) + c1, v11 = v10 + 16 * F_ * 2 - 1024, v12 = v10 + 4 * F_ * 2 - 2048, v13 = v10 + 20 * F_ * 2 - 3072;
    const unsigned v20 = krow * (D_ * 2) + c2, v21 = v20 + 16 * D_ * 2 - 1024, v22 = v20 + 4 * D_ * 2 - 2048, v23 = v20 + 20 * D_ * 2 - 3072;
    const i4 rw1 = mk_rsrc(w1, D_ * F_ * 2), rw2 = mk_rsrc(w2, D_ * F_ * 2);
#define KROT(i) ((i) + rot >= 14 ? (i) + rot - 14 : (i) + rot)
#define ISSUE(j) do { if ((j) < 14) dma4(ring + ((j) % RSL) * 4096, v10, v11, v12, v13, rw1, (unsigned)KROT(j) * (32 * F_ * 2)); \
                      else if ((j) < 28) dma4(ring + ((j) % RSL) * 4096, v20, v21, v22, v23, rw2, (unsigned)KROT((j) - 14) * (32 * D_ * 2)); } while (0)
    u64 tag = 0;
    if (wave < 7) { ISSUE(0); ISSUE(1); }
    else WAIT_VM(0);
    asm volatile("s_waitcnt vmcnt(8)" : "+v"(ur[0][0]), "+v"(ur[0][1]), "+v"(ur[0][2]), "+v"(ur[0][3]), "+v"(ur[1][0]), "+v"(ur[1][1]), "+v"(ur[1][2]), "+v"(ur[1][3]) :: "memory");
    if (wave == 7) { __builtin_amdgcn_s_setprio(3); tag = *(const u64*)(ws + WS_EPOCH) + 1ull; }
#pragma unroll
    for (int p = 0; p < 2; ++p) {
        const bool act = r16 < 14;
        const bf2v one2 = {(__bf16)1.0f, (__bf16)1.0f};
        float s = 0.f;
#pragma unroll
        for (int i = 0; i < 4; ++i)
#pragma unroll
            for (int e = 0; e < 4; ++e) { const unsigned w_ = ur[p][i][e]; s = __builtin_amdgcn_fdot2_f32_bf16(__builtin_bit_cast(bf2v, w_), one2, s, false); }
        s = row16_sum(act ? s : 0.f);
        const float mean = s * (1.0f / D_);
        const float mb = rbf(mean);
        unsigned av[4][4];
        f2 q2 = {0.f, 0.f};
#pragma unroll
        for (int i = 0; i < 4; ++i)
#pragma unroll
            for (int e = 0; e < 4; ++e) {
                f2 d = (f2){lo_f(ur[p][i][e]), hi_f(ur[p][i][e])} - (f2){mb, mb};
                q2 = __builtin_elementwise_fma(d, d, q2);
                av[i][e] = pk_bf16(d.x, d.y);
            }
#pragma unroll
        for (int i = 0; i < 4; ++i) asm volatile("" : "+v"(av[i][0]), "+v"(av[i][1]), "+v"(av[i][2]), "+v"(av[i][3]));
        const float q = row16_sum(act ? q2.x + q2.y : 0.f);
        const float dm = mean - mb;
        const float var = fmaxf(fmaf(-dm, dm, q * (1.0f / D_)), 0.f);
        const float sd = rbf(__builtin_amdgcn_sqrtf(rbf(rbf(var) + 1e-5f)));
        const float rinv = __builtin_amdgcn_rcpf(sd);
        const int lrow = 8 * wave + 4 * p + g;
        if (r16 == 0) *(unsigned*)(lds + ST_OFF + 4 * lrow) = (__float_as_uint(mb) >> 16) | (__float_as_uint(sd) & 0xffff0000u);
        char* xr = lds + lrow * 896;
        const int sw = (lrow >> 1) & 7;
#pragma unroll
        for (int i = 0; i < 4; ++i) {
            u4 o;
#pragma unroll
            for (int e = 0; e < 4; ++e) {
                f2 r = (f2){lo_f(av[i][e]), hi_f(av[i][e])} * (f2){rinv, rinv};
                o[e] = pk_bf16(r.x, r.y);
            }
            if (act) *(u4*)(xr + 16 * ((r16 + 14 * i) ^ sw)) = o;
        }
    }
    if (wave < 7) { ISSUE(2); }
    if (tid == 0) *(unsigned*)(lds + ST_OFF + 256) = 0u;
    RAW_BAR();
    const int q4 = r16 >> 2, p4 = lane & 3;
    if (wave == 7) {
        if (ks == 0) {
            __builtin_amdgcn_raw_buffer_store_b32(*(const unsigned*)(lds + ST_OFF + 4 * lane), rs, WS_ST + (64 * rt + lane) * 4, 0, 16);
            WAIT_VM(0);
            if (lane < NREP) __hip_atomic_store((gu64*)(ws + WS_FX) + lane * 64 + rt, tag, RLX_AGENT);
        }
        {
            const u4 ones = {0xffffffffu, 0xffffffffu, 0xffffffffu, 0xffffffffu};
            const unsigned pb = WS_PART + (unsigned)((rt * 4 + ks) * 56) * 1024 + lane * 16;
            for (int i = 0; i < 56; ++i) if (((i >> 1) & 3) != ks) __builtin_amdgcn_raw_buffer_store_b128(ones, rs, pb + i * 1024, 0, 16);
            __builtin_amdgcn_raw_buffer_store_b128(ones, rs, WS_PSC + (rt * 4 + ks) * 2048 + lane * 16, 0, 16);
            __builtin_amdgcn_raw_buffer_store_b128(ones, rs, WS_PSC + (rt * 4 + ks) * 2048 + 1024 + lane * 16, 0, 16);
            WAIT_VM(0);
            if (lane == 0) __hip_atomic_store((gu64*)(ws + WS_FP) + rt * 16 + ks, tag, RLX_AGENT);
        }
        __builtin_amdgcn_s_setprio(0);
        RAW_BAR();
        RAW_BAR();
        wait_flags((const gu64*)(ws + WS_FS) + (rt & (NREP - 1)) * 128 + lane, true, tag);
        {
            const gu64* p = lane < 48 ? (const gu64*)(ws + WS_FS) + (rt & (NREP - 1)) * 128 + 64 + lane : (const gu64*)(ws + WS_FP) + rt * 16 + (lane & 3);
            wait_flags(p, lane < 52, tag);
        }
        if (lane == 0) __hip_atomic_store((LAS unsigned*)((LAS char*)lds + ST_OFF + 256), 1u, __ATOMIC_RELAXED, __HIP_MEMORY_SCOPE_WORKGROUP);
        if (b == 0) {
#pragma unroll
            for (int i = 0; i < 2; ++i) wait_flags((const gu64*)(ws + WS_FP) + (16 * i + (lane >> 2)) * 16 + (lane & 3), true, tag);
            if (lane == 0) __hip_atomic_store((gu64*)(ws + WS_EPOCH), tag, RLX_AGENT);
        }
        return;
    }
    if (wave >= 4) __builtin_amdgcn_s_setprio(1);
    const int rr = 4 * g + q4;
    const int wtr = rr * 128 + (((rr >> 1) & 3) << 5) + 8 * p4;
    const char* rpb = lds + RING_OFF + wave * RING_W;
    const char* rp0 = rpb + wtr; const char* rp1 = rpb + (wtr ^ 32); const char* rp2 = rpb + (wtr ^ 64); const char* rp3 = rpb + (wtr ^ 96);
    const int asw = (r16 >> 1) & 7;
    const unsigned ae = (unsigned)((g ^ asw) * 16);
    const char* a0p = lds + r16 * 896;
    f4 acc[4][4];
#pragma unroll
    for (int m = 0; m < 4; ++m)
#pragma unroll
        for (int n = 0; n < 4; ++n) acc[m][n] = (f4){0, 0, 0, 0};
    u4 af[2][4]; u2 x[2][2][4];
#define LOADW(j, bsel) do { if ((j) <= 25) WAIT_VM(8); else if ((j) == 26) WAIT_VM(4); else WAIT_VM(0); \
        _Pragma("unroll") for (int hf = 0; hf < 2; ++hf) { const int so = ((j) % RSL) * 4096 + hf * 2048; \
            x[bsel][hf][0] = tr1(rp0 + so); x[bsel][hf][1] = tr1(rp1 + so); x[bsel][hf][2] = tr1(rp2 + so); x[bsel][hf][3] = tr1(rp3 + so); } } while (0)
#define LOADA(it, bsel) do { const char* ap = a0p + (ae ^ (unsigned)(KROT(it) << 6)); \
        _Pragma("unroll") for (int m = 0; m < 4; ++m) af[bsel][m] = *(const u4*)(ap + m * 16 * 896); } while (0)
    LOADW(0, 0); LOADA(0, 0);
    WAIT_LGKM0(); ISSUE(RSL);
#pragma unroll
    for (int it = 0; it < 14; ++it) {
        const int cur = it & 1, nxt = cur ^ 1;
        LOADW(it + 1, nxt);
        if (it < 13) LOADA(it + 1, nxt);
        __builtin_amdgcn_sched_barrier(0);
#pragma unroll
        for (int n = 0; n < 4; ++n) {
            const u4 wf = {x[cur][0][n].x, x[cur][0][n].y, x[cur][1][n].x, x[cur][1][n].y};
#pragma unroll
            for (int m = 0; m < 4; ++m) acc[m][n] = mfma_bf(wf, af[cur][m], acc[m][n]);
            if (n == 1) { __builtin_amdgcn_sched_barrier(0); WAIT_LGKM0(); ISSUE(it + 1 + RSL); __builtin_amdgcn_sched_barrier(0); }
        }
        __builtin_amdgcn_sched_barrier(0);
    }
    {
        u2 hp[4][4];
#pragma unroll
        for (int m = 0; m < 4; ++m)
#pragma unroll
            for (int n = 0; n < 4; ++n) {
                typedef __attribute__((ext_vector_type(2))) short s2v;
                const unsigned p0 = pk_bf16(acc[m][n][0], acc[m][n][1]), p1 = pk_bf16(acc[m][n][2], acc[m][n][3]);
                hp[m][n] = (u2){__builtin_bit_cast(unsigned, __builtin_elementwise_max(__builtin_bit_cast(s2v, p0), (s2v){0, 0})), __builtin_bit_cast(unsigned, __builtin_elementwise_max(__builtin_bit_cast(s2v, p1), (s2v){0, 0}))};
                acc[m][n] = (f4){0, 0, 0, 0};
            }
        __builtin_amdgcn_sched_barrier(0);
        RAW_BAR();
#pragma unroll
        for (int m = 0; m < 4; ++m)
#pragma unroll
            for (int n = 0; n < 4; ++n)
                *(u2*)(lds + (16 * m + r16) * 896 + 16 * ((8 * wave + 2 * n + (g >> 1)) ^ asw) + 8 * (g & 1)) = hp[m][n];
    }
    RAW_BAR();
    LOADA(0, 0);
#pragma unroll
    for (int it = 0; it < 14; ++it) {
        const int cur = it & 1, nxt = cur ^ 1;
        if (it < 13) { LOADW(14 + it + 1, nxt); LOADA(it + 1, nxt); }
        __builtin_amdgcn_sched_barrier(0);
#pragma unroll
        for (int n = 0; n < 4; ++n) {
            const u4 wf = {x[cur][0][n].x, x[cur][0][n].y, x[cur][1][n].x, x[cur][1][n].y};
#pragma unroll
            for (int m = 0; m < 4; ++m) acc[m][n] = mfma_bf(wf, af[cur][m], acc[m][n]);
            if (n == 1 && 14 + it + 1 + RSL < 28) { __builtin_amdgcn_sched_barrier(0); WAIT_LGKM0(); ISSUE(14 + it + 1 + RSL); __builtin_amdgcn_sched_barrier(0); }
        }
        __builtin_amdgcn_sched_barrier(0);
    }
    {
        float mx = 0.f;
#pragma unroll
        for (int m = 0; m < 4; ++m)
#pragma unroll
            for (int n = 0; n < 4; ++n)
#pragma unroll
                for (int e = 0; e < 4; ++e) mx = fmaxf(mx, fabsf(acc[m][n][e]));
        int e2 = (int)((__float_as_uint(mx) >> 23) & 255u) - 126;
        e2 = e2 < -100 ? -100 : e2;
        const float sc = __uint_as_float((unsigned)(127 + 15 - e2) << 23);
        const float scinv = __uint_as_float((unsigned)(127 - 15 + e2) << 23);
        const unsigned pbase = WS_PART + (unsigned)(((rt * 4 + ks) * 7 + wave) * 8) * 1024 + lane * 16;
#pragma unroll
        for (int m = 0; m < 4; ++m) {
            if (m != ks) {
#pragma unroll
                for (int np = 0; np < 2; ++np) {
                    u4 o = {pk_f16(acc[m][2 * np][0] * sc, acc[m][2 * np][1] * sc), pk_f16(acc[m][2 * np][2] * sc, acc[m][2 * np][3] * sc),
                            pk_f16(acc[m][2 * np + 1][0] * sc, acc[m][2 * np + 1][1] * sc), pk_f16(acc[m][2 * np + 1][2] * sc, acc[m][2 * np + 1][3] * sc)};
#pragma unroll
                    for (int e = 0; e < 4; ++e) o[e] = o[e] < 0xfffffffeu ? o[e] : 0xfffffffeu;
                    __builtin_amdgcn_raw_buffer_store_b128(o, rs, pbase + (2 * m + np) * 1024, 0, 16);
                }
            }
        }
        __builtin_amdgcn_raw_buffer_store_b32(__float_as_uint(scinv), rs, WS_PSC + (((rt * 4 + ks) * 8 + wave) * 64 + lane) * 4, 0, 16);
    }
    f4 own[4];
#pragma unroll
    for (int n = 0; n < 4; ++n) own[n] = ks == 0 ? acc[0][n] : ks == 1 ? acc[1][n] : ks == 2 ? acc[2][n] : acc[3][n];
    const int trow = 64 * rt + 16 * ks;
    const size_t uo = (size_t)(trow + (lane >> 3)) * D_ + 64 * wave + 8 * (lane & 7);
    const u4 uu0 = *(const u4*)(u + uo), uu1 = *(const u4*)(u + uo + 8 * D_);
    for (unsigned spins = 0; spins < (1u << 20); ++spins) {
        if (__hip_atomic_load((LAS unsigned*)((LAS char*)lds + ST_OFF + 256), __ATOMIC_RELAXED, __HIP_MEMORY_SCOPE_WORKGROUP)) break;
        __builtin_amdgcn_s_sleep(2);
    }
    asm volatile("" ::: "memory");
    {
        u2 sv[4];
#pragma unroll
        for (int n = 0; n < 4; ++n) sv[n] = __builtin_amdgcn_raw_buffer_load_b64(rs, WS_S + (16 * wave + 4 * n + g) * 16384 + (trow + r16) * 8, 0, 16);
        u4 pv[3][2]; float psc[3];
        for (unsigned spins = 0; spins < SPIN_CAP; ++spins) {
            unsigned mxv = 0;
#pragma unroll
            for (int j = 0; j < 3; ++j) {
                const int kp = (ks + 1 + j) & 3;
#pragma unroll
                for (int np = 0; np < 2; ++np)
                    pv[j][np] = __builtin_amdgcn_raw_buffer_load_b128(rs, WS_PART + (unsigned)((((rt * 4 + kp) * 7 + wave) * 8) + 2 * ks + np) * 1024 + lane * 16, 0, 16);
                psc[j] = __uint_as_float(__builtin_amdgcn_raw_buffer_load_b32(rs, WS_PSC + (((rt * 4 + kp) * 8 + wave) * 64 + lane) * 4, 0, 16));
            }
#pragma unroll
            for (int j = 0; j < 3; ++j) {
#pragma unroll
                for (int np = 0; np < 2; ++np)
#pragma unroll
                    for (int e = 0; e < 4; ++e) { const unsigned w_ = pv[j][np][e]; mxv = mxv > w_ ? mxv : w_; }
                const unsigned w_ = __float_as_uint(psc[j]); mxv = mxv > w_ ? mxv : w_;
            }
            if (!__any(mxv == 0xffffffffu)) break;
            __builtin_amdgcn_s_sleep(1);
            asm volatile("" ::: "memory");
        }
#pragma unroll
        for (int j = 0; j < 3; ++j)
#pragma unroll
            for (int n = 0; n < 4; ++n) {
                const unsigned w0 = pv[j][n >> 1][2 * (n & 1)], w1_ = pv[j][n >> 1][2 * (n & 1) + 1];
                own[n][0] = fmaf(lo_h(w0), psc[j], own[n][0]); own[n][1] = fmaf(hi_h(w0), psc[j], own[n][1]);
                own[n][2] = fmaf(lo_h(w1_), psc[j], own[n][2]); own[n][3] = fmaf(hi_h(w1_), psc[j], own[n][3]);
            }
        char* tb = lds + RING_OFF + wave * RING_W;
#pragma unroll
        for (int n = 0; n < 4; ++n) {
            float sg[4];
#pragma unroll
            for (int e = 0; e < 4; ++e) {
                const unsigned sw_ = sv[n][e >> 1];
                float s = (e & 1) ? hi_f(sw_) : lo_f(sw_);
                float o = rbf(own[n][e]);
                float gg = rbf(o * __builtin_amdgcn_rcpf(1.f + __expf(-o)));
                sg[e] = s * gg;
            }
            *(u2*)(tb + r16 * 144 + 32 * n + 8 * g) = (u2){pk_bf16(sg[0], sg[1]), pk_bf16(sg[2], sg[3])};
        }
#pragma unroll
        for (int h = 0; h < 2; ++h) {
            const u4 sgv = *(const u4*)(tb + (8 * h + (lane >> 3)) * 144 + 16 * (lane & 7));
            const u4 uv = h ? uu1 : uu0;
            u4 o;
#pragma unroll
            for (int e = 0; e < 4; ++e) { const unsigned a = uv[e], c = sgv[e]; o[e] = pk_bf16(lo_f(a) + lo_f(c), hi_f(a) + hi_f(c)); }
            *(u4*)(out + uo + (size_t)h * 8 * D_) = o;
        }
    }
}

__device__ __forceinline__ f2 cmul(f2 a, f2 b) {
    f2 t = a * b.xx;
    f2 by = b.yy; by.x = -by.x;
    return __builtin_elementwise_fma(a.yx, by, t);
}
__device__ __forceinline__ f2 zpow(float a2, float wv, float p) {
    float e = __builtin_amdgcn_exp2f(a2 * p);
    float th = __builtin_amdgcn_fractf(wv * p);
    return (f2){e * __builtin_amdgcn_cosf(th), e * __builtin_amdgcn_sinf(th)};
}
struct c2 { f2 a, b; };
__device__ __forceinline__ c2 cmul2(c2 x, c2 y) { c2 o; o.a = cmul(x.a, y.a); o.b = cmul(x.b, y.b); return o; }
__device__ __forceinline__ u2 pkc(c2 v) { return (u2){pk_f16(v.a.x, v.a.y), pk_f16(v.b.x, v.b.y)}; }
__device__ __forceinline__ u2 pkcn(c2 v) { return (u2){pk_f16(v.a.x, -v.a.y), pk_f16(v.b.x, -v.b.y)}; }
#define KS_ROW 528
#define KS_WAVE (16 * KS_ROW)
#define SSM_KS   0
#define SSM_XC   67584
#define SSM_KR   134144
#define SSM_KD   152576
#define SSM_BD   157824
#define SSM_RED  0

__device__ __forceinline__ void ssm_role(const uint16_t* __restrict__ u, const uint16_t* __restrict__ lar, const uint16_t* __restrict__ aim,
                                         const uint16_t* __restrict__ cr, const uint16_t* __restrict__ ci,
                                         const uint16_t* __restrict__ ldt, const uint16_t* __restrict__ dsk,
                                         char* ws, char* lds, int sb) {
    const int tid = threadIdx.x, lane = tid & 63, wave = __builtin_amdgcn_readfirstlane(tid >> 6);
    const int r16 = lane & 15, g = lane >> 4;
    const int c0 = 4 * sb;
    const int ch = wave >> 1;
    u64 tag = 0;
    const __amdgpu_buffer_rsrc_t rs = __builtin_amdgcn_make_buffer_rsrc(ws, 0, WS_BYTES, 0x00020000);
    u2 ucol[4];
    {
        const int c = c0 + ch, n0 = c * N_ + 128 * (wave & 1) + 2 * lane;
        const unsigned wl = *(const unsigned*)(lar + n0), wa = *(const unsigned*)(aim + n0), wr = *(const unsigned*)(cr + n0), wi = *(const unsigned*)(ci + n0);
        const uint16_t wdt = ldt[c];
        if (wave == 7) tag = *(const u64*)(ws + WS_EPOCH) + 1ull;
        const float dt = rbf(expf(bf2f(wdt)));
        const float ar0 = -rbf(expf(lo_f(wl))) * dt, ar1 = -rbf(expf(hi_f(wl))) * dt;
        const float ai0 = lo_f(wa) * dt, ai1 = hi_f(wa) * dt;
        const float Cr0 = lo_f(wr) * dt, Cr1 = hi_f(wr) * dt, Ci0 = lo_f(wi) * dt, Ci1 = hi_f(wi) * dt;
        float bd = row16_sum((fabsf(Cr0) + fabsf(Ci0)) + (fabsf(Cr1) + fabsf(Ci1)));
        bd += __shfl_xor(bd, 16); bd += __shfl_xor(bd, 32);
        int eb = (int)((__float_as_uint(bd) >> 23) & 255u) - 126;
        eb = eb < -100 ? -100 : eb;
        const float S1 = __uint_as_float((unsigned)(127 + 14 - eb) << 23);
        const float S1inv = __uint_as_float((unsigned)(127 + eb - 27) << 23);
        const float a20 = ar0 * 1.44269504088896f, wv0 = ai0 * 0.159154943091895f, a21 = ar1 * 1.44269504088896f, wv1 = ai1 * 0.159154943091895f;
        char* sc = lds + SSM_KS + wave * KS_WAVE;
        char* wp = sc + 8 * lane;
        const char* rp = sc + r16 * KS_ROW + 16 * g;
#define ZP(p) (c2){zpow(a20, wv0, p), zpow(a21, wv1, p)}
        c2 z1 = ZP(1.f), z8 = ZP(8.f), z64 = ZP(64.f), z128 = ZP(128.f), z1024 = ZP(1024.f);
#define WR(e, v) *(u2*)(wp + (e) * KS_ROW) = (v)
        u4 bfr[8];
        {
            c2 cj = {(f2){16384.f, 0.f}, (f2){16384.f, 0.f}};
#pragma unroll
            for (int j = 0; j < 8; ++j) {
                c2 hi = cmul2(cj, z64);
                WR(j, pkc(cj)); WR(8 + j, pkc(hi));
                if (j < 7) cj = cmul2(cj, z8);
            }
#pragma unroll
            for (int k4 = 0; k4 < 8; ++k4) bfr[k4] = *(const u4*)(rp + 64 * k4);
        }
        asm volatile("" :: "v"(bfr[0]) : "memory");
#pragma unroll
        for (int e = 0; e < 4; ++e) ucol[e] = *(const u2*)(u + (size_t)(4 * tid + e) * D_ + c0);
        c2 ze[8];
        ze[1] = z1; ze[2] = cmul2(z1, z1); ze[3] = cmul2(ze[2], z1); ze[4] = cmul2(ze[2], ze[2]);
        ze[5] = cmul2(ze[4], z1); ze[6] = cmul2(ze[4], ze[2]); ze[7] = cmul2(ze[4], ze[3]);
        c2 b = {(f2){Cr0 * S1, Ci0 * S1}, (f2){Cr1 * S1, Ci1 * S1}};
        c2 bh = cmul2(b, z1024);
        f4 acc[3];
#pragma unroll
        for (int i = 0; i < 2; ++i) {
            WR(8 * i, pkcn(b));
#pragma unroll
            for (int e = 1; e < 8; ++e) WR(8 * i + e, pkcn(cmul2(ze[e], b)));
            b = cmul2(b, z128);
        }
        acc[0] = (f4){0, 0, 0, 0};
#pragma unroll
        for (int k4 = 0; k4 < 8; ++k4) acc[0] = mfma_h(*(const u4*)(rp + 64 * k4), bfr[k4], acc[0]);
#pragma unroll
        for (int i = 0; i < 2; ++i) {
            WR(4 * i, pkcn(b));
#pragma unroll
            for (int e = 1; e < 4; ++e) WR(4 * i + e, pkcn(cmul2(ze[2 * e], b)));
            b = cmul2(b, z128);
        }
#pragma unroll
        for (int i = 0; i < 4; ++i) {
            WR(8 + 2 * i, pkcn(b));
            WR(8 + 2 * i + 1, pkcn(cmul2(b, ze[4])));
            if (i < 3) b = cmul2(b, z128);
        }
        acc[1] = (f4){0, 0, 0, 0};
#pragma unroll
        for (int k4 = 0; k4 < 8; ++k4) acc[1] = mfma_h(*(const u4*)(rp + 64 * k4), bfr[k4], acc[1]);
#pragma unroll
        for (int i = 0; i < 9; ++i) {
            WR(i, pkcn(bh));
            if (i < 8) bh = cmul2(bh, z128);
        }
        acc[2] = (f4){0, 0, 0, 0};
#pragma unroll
        for (int k4 = 0; k4 < 8; ++k4) acc[2] = mfma_h(*(const u4*)(rp + 64 * k4), bfr[k4], acc[2]);
        float* pd = (float*)sc;
        *(f4*)(pd + 128 * (g >> 1) + 4 * (g & 1) + 8 * r16) = acc[0] * S1inv;
        if (g < 2) *(f4*)(pd + 256 + 64 * g + 4 * r16) = acc[1] * S1inv;
        else {
            *(f2*)(pd + 384 + 64 * (g - 2) + 2 * r16) = (f2){acc[1][0], acc[1][1]} * S1inv;
            *(f2*)(pd + 384 + 64 * (g - 2) + 32 + 2 * r16) = (f2){acc[1][2], acc[1][3]} * S1inv;
        }
        if (g < 3) {
#pragma unroll
            for (int e = 0; e < 4; ++e) pd[512 + 16 * (4 * g + e) + r16] = acc[2][e] * S1inv;
        }
        if (lane == 0) *(float*)(lds + SSM_BD + 4 * wave) = bd;
    }
    __syncthreads();
    float SKinv[4];
    {
        const float* bdp = (const float*)(lds + SSM_BD);
        float SKs[4];
#pragma unroll
        for (int c = 0; c < 4; ++c) {
            float bb = 2.f * (bdp[2 * c] + bdp[2 * c + 1]);
            int e0 = (int)((__float_as_uint(bb) >> 23) & 255u) - 126; e0 = e0 < -100 ? -100 : e0;
            SKinv[c] = __uint_as_float((unsigned)(127 - 15 + e0) << 23); SKs[c] = __uint_as_float((unsigned)(127 + 15 - e0) << 23);
        }
        const int c = tid >> 7, q = tid & 127;
        const float SK = c == 0 ? SKs[0] : c == 1 ? SKs[1] : c == 2 ? SKs[2] : SKs[3];
#pragma unroll
        for (int rep = 0; rep < 2; ++rep) {
            const int qq = q + 128 * rep;
            if (qq < 164) {
                const char* s0 = lds + SSM_KS + (2 * c) * KS_WAVE + qq * 16;
                f4 v = (*(const f4*)s0 + *(const f4*)(s0 + KS_WAVE)) * SK;
                *(u2*)(lds + SSM_KD + c * 1312 + qq * 8) = (u2){pk_f16(v[0], v[1]), pk_f16(v[2], v[3])};
            }
        }
    }
    __syncthreads();
    {
        const int xa_i = tid & 255;
        int idx[8];
#pragma unroll
        for (int j = 0; j < 8; j += 2) {
            int l0 = 2047 - (8 * xa_i + j), l1 = l0 - 1;
            unsigned bits = pk_bf16((float)l0, (float)l1);
            idx[j] = l0 < 128 ? l0 : (int)((bits & 0xffffu) - 0x4280u);
            idx[j + 1] = l1 < 128 ? l1 : (int)((bits >> 16) - 0x4280u);
        }
#pragma unroll
        for (int rep = 0; rep < 2; ++rep) {
            const int c = (tid >> 8) + 2 * rep;
            const uint16_t* kd = (const uint16_t*)(lds + SSM_KD + c * 1312);
            u4 o;
#pragma unroll
            for (int j = 0; j < 4; ++j) o[j] = (unsigned)kd[idx[2 * j]] | ((unsigned)kd[idx[2 * j + 1]] << 16);
            char* kr = lds + SSM_KR + c * 4608;
            *(u4*)(kr + 16 * xa_i) = o;
            if (xa_i < 32) *(u4*)(kr + 4096 + 16 * xa_i) = (u4){0, 0, 0, 0};
        }
        if (tid < 128) { *(u2*)(lds + SSM_XC + (tid >> 5) * 16640 + ((tid >> 3) & 3) * 4160 + 8 * (tid & 7)) = (u2){0, 0}; }
    }
    if (wave == 7) wait_flags((const gu64*)(ws + WS_FX) + (sb & (NREP - 1)) * 64 + (lane & 31), true, tag);
    __syncthreads();
    {
        const u4 st = __builtin_amdgcn_raw_buffer_load_b128(rs, WS_ST + tid * 16, 0, 16);
        float xv[4][4];
#pragma unroll
        for (int e = 0; e < 4; ++e) {
            const unsigned sw_ = st[e];
            const float mb = lo_f(sw_), rinv = __builtin_amdgcn_rcpf(hi_f(sw_));
#pragma unroll
            for (int h = 0; h < 2; ++h) {
                const unsigned uw = ucol[e][h];
                unsigned a = pk_bf16(lo_f(uw) - mb, hi_f(uw) - mb);
                unsigned r = pk_bf16(lo_f(a) * rinv, hi_f(a) * rinv);
                xv[2 * h][e] = lo_f(r); xv[2 * h + 1][e] = hi_f(r);
            }
        }
#pragma unroll
        for (int cc = 0; cc < 4; ++cc) {
            const unsigned d0 = pk_f16(xv[cc][0], xv[cc][1]), d1 = pk_f16(xv[cc][2], xv[cc][3]);
            const unsigned n0 = __shfl_down(d0, 1), n1 = __shfl_down(d1, 1);
            const unsigned mid = __builtin_amdgcn_alignbyte(d1, d0, 2), e1 = __builtin_amdgcn_alignbyte(n0, d1, 2), e2 = __builtin_amdgcn_alignbyte(n1, n0, 2);
            char* xc = lds + SSM_XC + cc * 16640 + 8 * (tid + 8);
            *(u2*)(xc) = (u2){d0, d1};
            if (lane < 63) {
                *(u2*)(xc + 4160) = (u2){mid, e1};
                *(u2*)(xc + 2 * 4160) = (u2){d1, n0};
                *(u2*)(xc + 3 * 4160) = (u2){e1, e2};
            } else {
                *(unsigned*)(xc + 4160) = mid; *(uint16_t*)(xc + 4160 + 4) = (uint16_t)(d1 >> 16);
                *(unsigned*)(xc + 2 * 4160) = d1;
                *(uint16_t*)(xc + 3 * 4160) = (uint16_t)(d1 >> 16);
            }
            if (lane == 0) {
                *(uint16_t*)(xc + 4160 - 2) = (uint16_t)d0;
                *(unsigned*)(xc + 2 * 4160 - 4) = d0;
                *(uint16_t*)(xc + 3 * 4160 - 6) = (uint16_t)d0; *(unsigned*)(xc + 3 * 4160 - 4) = mid;
            }
        }
    }
    __syncthreads();
    f4 acc[8];
#pragma unroll
    for (int n = 0; n < 8; ++n) acc[n] = (f4){0, 0, 0, 0};
    {
        const int rh = wave & 1;
        const char* xc = lds + SSM_XC + ch * 16640 + ((r16 + 1) & 3) * 4160 + 8 * (10 - 2 * g + ((r16 + 1) >> 2)) + 64 * 4 * rh;
        const char* kr = lds + SSM_KR + ch * 4608 + 16 * (257 - 2 * r16 - g) + 64 * 4 * rh;
#pragma unroll
        for (int rr = 0; rr < 4; ++rr) {
            u4 af[8], bfv[8];
#pragma unroll
            for (int kp = 0; kp < 8; ++kp) {
                const char* p = xc + 64 * (8 * kp + rr);
                u2 a = *(const u2*)p, b = *(const u2*)(p + 8);
                af[kp] = (u4){a.x, a.y, b.x, b.y};
            }
#pragma unroll
            for (int j = 0; j < 8; ++j) bfv[j] = *(const u4*)(kr - 64 * (8 * j - rr));
#pragma unroll
            for (int n = 0; n < 8; ++n)
#pragma unroll
                for (int kp = 0; kp <= n; ++kp) acc[n] = mfma_h(af[kp], bfv[n - kp], acc[n]);
        }
    }
    {
        char* red = lds + SSM_RED;
#pragma unroll
        for (int n = 0; n < 8; ++n) *(f4*)(red + ((wave * 8 + n) * 64 + lane) * 16) = acc[n];
    }
    __syncthreads();
    {
        const char* red = lds + SSM_RED;
        const int t = 256 * wave + 16 * r16 + 4 * g;
        float s[4][4];
#pragma unroll
        for (int c = 0; c < 4; ++c) {
            f4 y = *(const f4*)(red + (((2 * c) * 8 + wave) * 64 + lane) * 16) + *(const f4*)(red + (((2 * c + 1) * 8 + wave) * 64 + lane) * 16);
            const u2 xx = *(const u2*)(lds + SSM_XC + c * 16640 + 2 * (t + 32));
            const float ds = bf2f(dsk[c0 + c]);
#pragma unroll
            for (int e = 0; e < 4; ++e) {
                const unsigned xw = xx[e >> 1];
                float x = (e & 1) ? hi_h(xw) : lo_h(xw);
                s[c][e] = rbf(y[e] * SKinv[c]) + rbf(x * ds);
            }
        }
        const unsigned soff = WS_S + sb * 16384 + t * 8;
        __builtin_amdgcn_raw_buffer_store_b128((u4){pk_bf16(s[0][0], s[1][0]), pk_bf16(s[2][0], s[3][0]), pk_bf16(s[0][1], s[1][1]), pk_bf16(s[2][1], s[3][1])}, rs, soff, 0, 16);
        __builtin_amdgcn_raw_buffer_store_b128((u4){pk_bf16(s[0][2], s[1][2]), pk_bf16(s[2][2], s[3][2]), pk_bf16(s[0][3], s[1][3]), pk_bf16(s[2][3], s[3][3])}, rs, soff + 16, 0, 16);
    }
    WAIT_VM(0);
    __syncthreads();
    if (tid >= 448 && tid < 448 + NREP) __hip_atomic_store((gu64*)(ws + WS_FS) + (tid - 448) * 128 + sb, tag, RLX_AGENT);
}

__global__ __launch_bounds__(512, 2) void fused_kernel(const uint16_t* __restrict__ u, const uint16_t* __restrict__ lar, const uint16_t* __restrict__ aim,
                                                       const uint16_t* __restrict__ cr, const uint16_t* __restrict__ ci,
                                                       const uint16_t* __restrict__ ldt, const uint16_t* __restrict__ dsk,
                                                       const uint16_t* __restrict__ w1, const uint16_t* __restrict__ w2,
                                                       char* ws, uint16_t* __restrict__ out) {
    extern __shared__ __attribute__((aligned(16))) char lds[];
    const int b = blockIdx.x;
    if (b < NFFN) ffn_role(u, w1, w2, ws, out, lds, b);
    else ssm_role(u, lar, aim, cr, ci, ldt, dsk, ws, lds, b - NFFN);
}

extern "C" void kernel_launch(void* const* d_in, const int* in_sizes, int n_in,
                              void* d_out, int out_size, void* d_ws, size_t ws_size,
                              hipStream_t stream) {
    (void)hipFuncSetAttribute((const void*)fused_kernel, hipFuncAttributeMaxDynamicSharedMemorySize, LDS_BYTES);
    fused_kernel<<<NFFN + NSSM, 512, LDS_BYTES, stream>>>((const uint16_t*)d_in[0], (const uint16_t*)d_in[1], (const uint16_t*)d_in[2], (const uint16_t*)d_in[3],
                                                  (const uint16_t*)d_in[4], (const uint16_t*)d_in[5], (const uint16_t*)d_in[6], (const uint16_t*)d_in[8],
                                                  (const uint16_t*)d_in[9], (char*)d_ws, (uint16_t*)d_out);
}
```

```cpp
#include <hip/hip_runtime.h>
#include <stdint.h>

#define L_ 2048
#define D_ 448
#define N_ 256
#define F_ 1792

typedef __attribute__((ext_vector_type(8))) __bf16 bf8;
typedef __attribute__((ext_vector_type(8))) _Float16 h8;
typedef __attribute__((ext_vector_type(4))) short s4v;
typedef __attribute__((ext_vector_type(4))) float f4;
typedef __attribute__((ext_vector_type(2))) float f2;
typedef __attribute__((ext_vector_type(4))) unsigned u4;
typedef __attribute__((ext_vector_type(2))) unsigned u2;
typedef __attribute__((ext_vector_type(2))) __bf16 bf2v;
typedef __attribute__((ext_vector_type(2))) _Float16 h2v;
typedef unsigned long long u64;
#define LAS __attribute__((address_space(3)))
#define GAS __attribute__((address_space(1)))
typedef GAS u64 gu64;
#define RLX_AGENT __ATOMIC_RELAXED, __HIP_MEMORY_SCOPE_AGENT

__device__ __forceinline__ float bf2f(uint16_t v) { return __uint_as_float(((uint32_t)v) << 16); }
__device__ __forceinline__ unsigned pk_bf16(float a, float b) { bf2v r; r.x = (__bf16)a; r.y = (__bf16)b; return __builtin_bit_cast(unsigned, r); }
__device__ __forceinline__ unsigned pk_f16(float a, float b) { h2v r; r.x = (_Float16)a; r.y = (_Float16)b; return __builtin_bit_cast(unsigned, r); }
__device__ __forceinline__ float rbf(float f) { return __uint_as_float(pk_bf16(f, 0.f) << 16); }
__device__ __forceinline__ float lo_f(unsigned w) { return __uint_as_float(w << 16); }
__device__ __forceinline__ float hi_f(unsigned w) { return __uint_as_float(w & 0xffff0000u); }
__device__ __forceinline__ float lo_h(unsigned w) { return (float)__builtin_bit_cast(h2v, w).x; }
__device__ __forceinline__ float hi_h(unsigned w) { return (float)__builtin_bit_cast(h2v, w).y; }
__device__ __forceinline__ f4 mfma_bf(u4 a, u4 b, f4 c) { return __builtin_amdgcn_mfma_f32_16x16x32_bf16(__builtin_bit_cast(bf8, a), __builtin_bit_cast(bf8, b), c, 0, 0, 0); }
__device__ __forceinline__ f4 mfma_h(u4 a, u4 b, f4 c) { return __builtin_amdgcn_mfma_f32_16x16x32_f16(__builtin_bit_cast(h8, a), __builtin_bit_cast(h8, b), c, 0, 0, 0); }
template <int CTRL> __device__ __forceinline__ float dpp_f(float v) { return __int_as_float(__builtin_amdgcn_update_dpp(0, __float_as_int(v), CTRL, 0xf, 0xf, false)); }
__device__ __forceinline__ float row16_sum(float v) { v += dpp_f<0x128>(v); v += dpp_f<0x124>(v); v += dpp_f<0x122>(v); v += dpp_f<0x121>(v); return v; }
__device__ __forceinline__ float row16_max(float v) { v = fmaxf(v, dpp_f<0x128>(v)); v = fmaxf(v, dpp_f<0x124>(v)); v = fmaxf(v, dpp_f<0x122>(v)); v = fmaxf(v, dpp_f<0x121>(v)); return v; }
__device__ __forceinline__ u2 tr1(const char* p) { return __builtin_bit_cast(u2, __builtin_amdgcn_ds_read_tr16_b64_v4i16((LAS s4v*)p)); }
__device__ __forceinline__ void dma_pair(unsigned dst, unsigned va, unsigned vb, const void* sbase) {
    asm volatile("s_mov_b32 m0, %0\n\ts_nop 0\n\tglobal_load_lds_dwordx4 %1, %3\n\tglobal_load_lds_dwordx4 %2, %3 offset:1024" :: "s"(dst), "v"(va), "v"(vb), "s"(sbase) : "memory");
}
#define WAIT_VM(n) asm volatile("s_waitcnt vmcnt(" #n ")" ::: "memory")
#define WAIT_LGKM0() asm volatile("s_waitcnt lgkmcnt(0)" ::: "memory")
#define RAW_BAR() do { WAIT_LGKM0(); __builtin_amdgcn_s_barrier(); asm volatile("" ::: "memory"); } while (0)

#define SLEEPN 4

#define NFFN 128
#define NSSM 112
#define WS_EPOCH 0
#define NREP 16
#define WS_FX    65536
#define WS_ST    32768
#define WS_FS    131072
#define WS_FP    196608
#define WS_PSC   262144
#define WS_S     (3u << 20)
#define WS_PART  (5u << 20)
#define WS_BYTES (16u << 20)
#define LDS_BYTES 160000
#define SPIN_CAP (1u << 14)

__device__ __forceinline__ void wait_flags(const gu64* p, bool active, u64 tag) {
    for (unsigned spins = 0; spins < SPIN_CAP; ++spins) {
        u64 v = active ? __hip_atomic_load(p, RLX_AGENT) : tag;
        if (__all(v == tag)) break;
        __builtin_amdgcn_s_sleep(SLEEPN);
    }
    __builtin_amdgcn_fence(__ATOMIC_ACQUIRE, "wavefront");
}

#define XN_BYTES 57344
#define RING_OFF XN_BYTES
#define RSL 7
#define RING_W (RSL * 2048)
#define ST_OFF (RING_OFF + 7 * RING_W)
__device__ __forceinline__ void ffn_role(const uint16_t* __restrict__ u, const uint16_t* __restrict__ w1, const uint16_t* __restrict__ w2,
                                         char* ws, uint16_t* __restrict__ out, char* lds, int b) {
    const int tid = threadIdx.x, lane = tid & 63, wave = __builtin_amdgcn_readfirstlane(tid >> 6);
    const int r16 = lane & 15, g = lane >> 4;
    const int xcd = b & 7, ks = xcd & 3, rt = 16 * (xcd >> 2) + (b >> 3);
    const int rot = (b >> 3) % 14;
    const __amdgpu_buffer_rsrc_t rs = __builtin_amdgcn_make_buffer_rsrc(ws, 0, WS_BYTES, 0x00020000);
    u4 ur[2][4];
    {
        const u4* up = (const u4*)(u + (size_t)(64 * rt + 8 * wave + g) * D_) + (r16 < 14 ? r16 : 13);
        const u4* up2 = up + 4 * 56;
        asm volatile("global_load_dwordx4 %0, %8, off\n\tglobal_load_dwordx4 %1, %8, off offset:224\n\tglobal_load_dwordx4 %2, %8, off offset:448\n\tglobal_load_dwordx4 %3, %8, off offset:672\n\t"
                     "global_load_dwordx4 %4, %9, off\n\tglobal_load_dwordx4 %5, %9, off offset:224\n\tglobal_load_dwordx4 %6, %9, off offset:448\n\tglobal_load_dwordx4 %7, %9, off offset:672"
                     : "=&v"(ur[0][0]), "=&v"(ur[0][1]), "=&v"(ur[0][2]), "=&v"(ur[0][3]), "=&v"(ur[1][0]), "=&v"(ur[1][1]), "=&v"(ur[1][2]), "=&v"(ur[1][3])
                     : "v"(up), "v"(up2) : "memory");
    }
    const unsigned ldsbase = (unsigned)(size_t)(LAS char*)lds;
    const unsigned ring = ldsbase + RING_OFF + wave * RING_W;
    const int rho = lane >> 3;
    const unsigned srcc = (unsigned)(((lane & 7) ^ (((rho >> 1) & 3) << 1)) * 16);
    const int krow = 8 * (rho >> 2) + (rho & 3);
    const unsigned v1a = krow * (F_ * 2) + srcc, v1b = v1a + 16 * F_ * 2 - 1024;
    const unsigned v2a = krow * (D_ * 2) + srcc, v2b = v2a + 16 * D_ * 2 - 1024;
    const char* w1b = (const char*)(w1 + 448 * ks + 64 * wave);
    const char* w2b = (const char*)(w2 + (size_t)448 * ks * D_ + 64 * wave);
#define KROT(i) ((i) + rot >= 14 ? (i) + rot - 14 : (i) + rot)
#define ISSUE(s) do { if ((s) < 28) dma_pair(ring + ((s) % RSL) * 2048, v1a, v1b, w1b + (size_t)KROT((s) >> 1) * (32 * F_ * 2) + ((s) & 1) * (4 * F_ * 2)); \
                      else if ((s) < 56) dma_pair(ring + ((s) % RSL) * 2048, v2a, v2b, w2b + (size_t)KROT(((s) - 28) >> 1) * (32 * D_ * 2) + ((s) & 1) * (4 * D_ * 2)); } while (0)
    u64 tag = 0;
    if (wave < 7) { ISSUE(0); ISSUE(1); ISSUE(2); ISSUE(3); }
    else WAIT_VM(0);
    asm volatile("s_waitcnt vmcnt(8)" : "+v"(ur[0][0]), "+v"(ur[0][1]), "+v"(ur[0][2]), "+v"(ur[0][3]), "+v"(ur[1][0]), "+v"(ur[1][1]), "+v"(ur[1][2]), "+v"(ur[1][3]) :: "memory");
    if (wave == 7) { __builtin_amdgcn_s_setprio(3); tag = *(const u64*)(ws + WS_EPOCH) + 1ull; }
#pragma unroll
    for (int p = 0; p < 2; ++p) {
        const bool act = r16 < 14;
        const bf2v one2 = {(__bf16)1.0f, (__bf16)1.0f};
        float s = 0.f;
#pragma unroll
        for (int i = 0; i < 4; ++i)
#pragma unroll
            for (int e = 0; e < 4; ++e) { const unsigned w_ = ur[p][i][e]; s = __builtin_amdgcn_fdot2_f32_bf16(__builtin_bit_cast(bf2v, w_), one2, s, false); }
        s = row16_sum(act ? s : 0.f);
        const float mean = s * (1.0f / D_);
        const float mb = rbf(mean);
        unsigned av[4][4];
        f2 q2 = {0.f, 0.f};
#pragma unroll
        for (int i = 0; i < 4; ++i)
#pragma unroll
            for (int e = 0; e < 4; ++e) {
                f2 d = (f2){lo_f(ur[p][i][e]), hi_f(ur[p][i][e])} - (f2){mb, mb};
                q2 = __builtin_elementwise_fma(d, d, q2);
                av[i][e] = pk_bf16(d.x, d.y);
            }
#pragma unroll
        for (int i = 0; i < 4; ++i) asm volatile("" : "+v"(av[i][0]), "+v"(av[i][1]), "+v"(av[i][2]), "+v"(av[i][3]));
        const float q = row16_sum(act ? q2.x + q2.y : 0.f);
        const float dm = mean - mb;
        const float var = fmaxf(fmaf(-dm, dm, q * (1.0f / D_)), 0.f);
        const float sd = rbf(__builtin_amdgcn_sqrtf(rbf(rbf(var) + 1e-5f)));
        const float rinv = __builtin_amdgcn_rcpf(sd);
        const int lrow = 8 * wave + 4 * p + g;
        if (r16 == 0) *(unsigned*)(lds + ST_OFF + 4 * lrow) = (__float_as_uint(mb) >> 16) | (__float_as_uint(sd) & 0xffff0000u);
        char* xr = lds + lrow * 896;
        const int sw = (lrow >> 1) & 7;
#pragma unroll
        for (int i = 0; i < 4; ++i) {
            u4 o;
#pragma unroll
            for (int e = 0; e < 4; ++e) {
                f2 r = (f2){lo_f(av[i][e]), hi_f(av[i][e])} * (f2){rinv, rinv};
                o[e] = pk_bf16(r.x, r.y);
            }
            if (act) *(u4*)(xr + 16 * ((r16 + 14 * i) ^ sw)) = o;
        }
    }
    if (wave < 7) { ISSUE(4); ISSUE(5); ISSUE(6); }
    if (tid == 0) *(unsigned*)(lds + ST_OFF + 256) = 0u;
    RAW_BAR();
    const int q4 = r16 >> 2, p4 = lane & 3;
    if (wave == 7) {
        if (ks == 0) {
            __builtin_amdgcn_raw_buffer_store_b32(*(const unsigned*)(lds + ST_OFF + 4 * lane), rs, WS_ST + (64 * rt + lane) * 4, 0, 16);
            WAIT_VM(0);
            if (lane < NREP) __hip_atomic_store((gu64*)(ws + WS_FX) + lane * 64 + rt, tag, RLX_AGENT);
        }
        if (lane == 0) __hip_atomic_store((gu64*)(ws + WS_FP) + rt * 16 + ks, tag, RLX_AGENT);
        wait_flags((const gu64*)(ws + WS_FP) + rt * 16 + 4, true, tag);
        __builtin_amdgcn_s_setprio(0);
        RAW_BAR();
        RAW_BAR();
        __builtin_amdgcn_s_setprio(3);
        {
            const gu64* p0 = (const gu64*)(ws + WS_FS) + (rt & (NREP - 1)) * 128 + lane;
            const gu64* p1 = p0 + (lane < 48 ? 64 : 0);
            for (unsigned spins = 0; spins < SPIN_CAP; ++spins) {
                const u64 a = __hip_atomic_load(p0, RLX_AGENT), c = __hip_atomic_load(p1, RLX_AGENT);
                if (__all(a == tag && c == tag)) break;
                __builtin_amdgcn_s_sleep(SLEEPN);
            }
            __builtin_amdgcn_fence(__ATOMIC_ACQUIRE, "wavefront");
        }
        if (lane == 0) __hip_atomic_store((LAS unsigned*)((LAS char*)lds + ST_OFF + 256), 1u, __ATOMIC_RELAXED, __HIP_MEMORY_SCOPE_WORKGROUP);
        if (b == 0) {
#pragma unroll
            for (int i = 0; i < 4; ++i) wait_flags((const gu64*)(ws + WS_FP) + (8 * i + (lane >> 3)) * 16 + (lane & 7), (lane & 7) < 5, tag);
            if (lane == 0) __hip_atomic_store((gu64*)(ws + WS_EPOCH), tag, RLX_AGENT);
        }
        return;
    }
    if (wave >= 4) __builtin_amdgcn_s_setprio(1);
    const int rr = 4 * g + q4;
    const int wtr = rr * 128 + (((rr >> 1) & 3) << 5) + 8 * p4;
    const char* rpb = lds + RING_OFF + wave * RING_W;
    const char* rp0 = rpb + wtr; const char* rp1 = rpb + (wtr ^ 32); const char* rp2 = rpb + (wtr ^ 64); const char* rp3 = rpb + (wtr ^ 96);
    const int asw = (r16 >> 1) & 7;
    const unsigned ae = (unsigned)((g ^ asw) * 16);
    const char* a0p = lds + r16 * 896;
    f4 acc[4][4];
#pragma unroll
    for (int m = 0; m < 4; ++m)
#pragma unroll
        for (int n = 0; n < 4; ++n) acc[m][n] = (f4){0, 0, 0, 0};
    u4 af[2][4]; u2 x[2][2][4];
#define VMW(n) do { if ((n) >= 10) WAIT_VM(10); else if ((n) >= 8) WAIT_VM(8); else if ((n) >= 6) WAIT_VM(6); else if ((n) >= 4) WAIT_VM(4); else if ((n) >= 2) WAIT_VM(2); else WAIT_VM(0); } while (0)
#define LOADW(j, bsel, issued) do { VMW(2 * ((issued) - (2 * (j) + 1))); \
        _Pragma("unroll") for (int hf = 0; hf < 2; ++hf) { const int so = ((2 * (j) + hf) % RSL) * 2048; \
            x[bsel][hf][0] = tr1(rp0 + so); x[bsel][hf][1] = tr1(rp1 + so); x[bsel][hf][2] = tr1(rp2 + so); x[bsel][hf][3] = tr1(rp3 + so); } } while (0)
#define LOADA(it, bsel) do { const char* ap = a0p + (ae ^ (unsigned)(KROT(it) << 6)); \
        _Pragma("unroll") for (int m = 0; m < 4; ++m) af[bsel][m] = *(const u4*)(ap + m * 16 * 896); } while (0)
#define REFILL(j) do { WAIT_LGKM0(); ISSUE(2 * (j) + RSL); ISSUE(2 * (j) + 1 + RSL); } while (0)
#define ISSUED(j) ((2 * (j) + 1 + RSL) < 55 ? (2 * (j) + 1 + RSL) : 55)
    LOADW(0, 0, RSL - 1); LOADA(0, 0);
    REFILL(0);
#pragma unroll
    for (int it = 0; it < 14; ++it) {
        const int cur = it & 1, nxt = cur ^ 1;
        LOADW(it + 1, nxt, ISSUED(it));
        if (it < 13) LOADA(it + 1, nxt);
        __builtin_amdgcn_sched_barrier(0);
#pragma unroll
        for (int n = 0; n < 4; ++n) {
            const u4 wf = {x[cur][0][n].x, x[cur][0][n].y, x[cur][1][n].x, x[cur][1][n].y};
#pragma unroll
            for (int m = 0; m < 4; ++m) acc[m][n] = mfma_bf(wf, af[cur][m], acc[m][n]);
            __builtin_amdgcn_sched_barrier(0);
            if (n == 1) { WAIT_LGKM0(); ISSUE(2 * (it + 1) + RSL); }
            if (n == 2) { ISSUE(2 * (it + 1) + 1 + RSL); }
            __builtin_amdgcn_sched_barrier(0);
        }
    }
    {
        u2 hp[4][4];
#pragma unroll
        for (int m = 0; m < 4; ++m)
#pragma unroll
            for (int n = 0; n < 4; ++n) {
                typedef __attribute__((ext_vector_type(2))) short s2v;
                const unsigned p0 = pk_bf16(acc[m][n][0], acc[m][n][1]), p1 = pk_bf16(acc[m][n][2], acc[m][n][3]);
                hp[m][n] = (u2){__builtin_bit_cast(unsigned, __builtin_elementwise_max(__builtin_bit_cast(s2v, p0), (s2v){0, 0})), __builtin_bit_cast(unsigned, __builtin_elementwise_max(__builtin_bit_cast(s2v, p1), (s2v){0, 0}))};
                acc[m][n] = (f4){0, 0, 0, 0};
            }
        __builtin_amdgcn_sched_barrier(0);
        RAW_BAR();
#pragma unroll
        for (int m = 0; m < 4; ++m)
#pragma unroll
            for (int n = 0; n < 4; ++n)
                *(u2*)(lds + (16 * m + r16) * 896 + 16 * ((8 * wave + 2 * n + (g >> 1)) ^ asw) + 8 * (g & 1)) = hp[m][n];
    }
    RAW_BAR();
    LOADA(0, 0);
#pragma unroll
    for (int it = 0; it < 14; ++it) {
        const int cur = it & 1, nxt = cur ^ 1;
        if (it < 13) { LOADW(14 + it + 1, nxt, ISSUED(14 + it)); LOADA(it + 1, nxt); }
        __builtin_amdgcn_sched_barrier(0);
#pragma unroll
        for (int n = 0; n < 4; ++n) {
            const u4 wf = {x[cur][0][n].x, x[cur][0][n].y, x[cur][1][n].x, x[cur][1][n].y};
#pragma unroll
            for (int m = 0; m < 4; ++m) acc[m][n] = mfma_bf(wf, af[cur][m], acc[m][n]);
            __builtin_amdgcn_sched_barrier(0);
            if (n == 1 && 2 * (14 + it + 1) + RSL < 56) { WAIT_LGKM0(); ISSUE(2 * (14 + it + 1) + RSL); }
            if (n == 2 && 2 * (14 + it + 1) + RSL < 56) { ISSUE(2 * (14 + it + 1) + 1 + RSL); }
            __builtin_amdgcn_sched_barrier(0);
        }
    }
    {
        float mx = 0.f;
#pragma unroll
        for (int m = 0; m < 4; ++m)
#pragma unroll
            for (int n = 0; n < 4; ++n)
#pragma unroll
                for (int e = 0; e < 4; ++e) mx = fmaxf(mx, fabsf(acc[m][n][e]));
        int e2 = (int)((__float_as_uint(mx) >> 23) & 255u) - 126;
        e2 = e2 < -100 ? -100 : e2;
        const float sc = __uint_as_float((unsigned)(127 + 15 - e2) << 23);
        const float scinv = __uint_as_float((unsigned)(127 - 15 + e2) << 23);
        const unsigned pbase = WS_PART + (unsigned)(((rt * 4 + ks) * 7 + wave) * 8) * 1024 + lane * 16;
#pragma unroll
        for (int m = 0; m < 4; ++m) {
            if (m != ks) {
#pragma unroll
                for (int np = 0; np < 2; ++np) {
                    u4 o = {pk_f16(acc[m][2 * np][0] * sc, acc[m][2 * np][1] * sc), pk_f16(acc[m][2 * np][2] * sc, acc[m][2 * np][3] * sc),
                            pk_f16(acc[m][2 * np + 1][0] * sc, acc[m][2 * np + 1][1] * sc), pk_f16(acc[m][2 * np + 1][2] * sc, acc[m][2 * np + 1][3] * sc)};
#pragma unroll
                    for (int e = 0; e < 4; ++e) o[e] = o[e] < 0xfffffffeu ? o[e] : 0xfffffffeu;
                    __builtin_amdgcn_raw_buffer_store_b128(o, rs, pbase + (2 * m + np) * 1024, 0, 16);
                }
            }
        }
        __builtin_amdgcn_raw_buffer_store_b32(__float_as_uint(scinv), rs, WS_PSC + (((rt * 4 + ks) * 8 + wave) * 64 + lane) * 4, 0, 16);
    }
    f4 own[4];
#pragma unroll
    for (int n = 0; n < 4; ++n) own[n] = ks == 0 ? acc[0][n] : ks == 1 ? acc[1][n] : ks == 2 ? acc[2][n] : acc[3][n];
    const int trow = 64 * rt + 16 * ks;
    const size_t uo = (size_t)(trow + (lane >> 3)) * D_ + 64 * wave + 8 * (lane & 7);
    const u4 uu0 = *(const u4*)(u + uo), uu1 = *(const u4*)(u + uo + 8 * D_);
    for (unsigned spins = 0; spins < (1u << 20); ++spins) {
        if (__hip_atomic_load((LAS unsigned*)((LAS char*)lds + ST_OFF + 256), __ATOMIC_RELAXED, __HIP_MEMORY_SCOPE_WORKGROUP)) break;
        __builtin_amdgcn_s_sleep(2);
    }
    asm volatile("" ::: "memory");
    {
        u2 sv[4];
#pragma unroll
        for (int n = 0; n < 4; ++n) sv[n] = __builtin_amdgcn_raw_buffer_load_b64(rs, WS_S + (16 * wave + 4 * n + g) * 16384 + (trow + r16) * 8, 0, 16);
        u4 pv[3][2]; float psc[3];
        for (unsigned spins = 0; spins < SPIN_CAP; ++spins) {
            unsigned mxv = 0;
#pragma unroll
            for (int j = 0; j < 3; ++j) {
                const int kp = (ks + 1 + j) & 3;
#pragma unroll
                for (int np = 0; np < 2; ++np)
                    pv[j][np] = __builtin_amdgcn_raw_buffer_load_b128(rs, WS_PART + (unsigned)((((rt * 4 + kp) * 7 + wave) * 8) + 2 * ks + np) * 1024 + lane * 16, 0, 16);
                psc[j] = __uint_as_float(__builtin_amdgcn_raw_buffer_load_b32(rs, WS_PSC + (((rt * 4 + kp) * 8 + wave) * 64 + lane) * 4, 0, 16));
            }
#pragma unroll
            for (int j = 0; j < 3; ++j) {
#pragma unroll
                for (int np = 0; np < 2; ++np)
#pragma unroll
                    for (int e = 0; e < 4; ++e) { const unsigned w_ = pv[j][np][e]; mxv = mxv > w_ ? mxv : w_; }
                const unsigned w_ = __float_as_uint(psc[j]); mxv = mxv > w_ ? mxv : w_;
            }
            if (!__any(mxv == 0xffffffffu)) break;
            __builtin_amdgcn_s_sleep(1);
            asm volatile("" ::: "memory");
        }
#pragma unroll
        for (int j = 0; j < 3; ++j)
#pragma unroll
            for (int n = 0; n < 4; ++n) {
                const unsigned w0 = pv[j][n >> 1][2 * (n & 1)], w1_ = pv[j][n >> 1][2 * (n & 1) + 1];
                own[n][0] = fmaf(lo_h(w0), psc[j], own[n][0]); own[n][1] = fmaf(hi_h(w0), psc[j], own[n][1]);
                own[n][2] = fmaf(lo_h(w1_), psc[j], own[n][2]); own[n][3] = fmaf(hi_h(w1_), psc[j], own[n][3]);
            }
        char* tb = lds + RING_OFF + wave * RING_W;
#pragma unroll
        for (int n = 0; n < 4; ++n) {
            float sg[4];
#pragma unroll
            for (int e = 0; e < 4; ++e) {
                const unsigned sw_ = sv[n][e >> 1];
                float s = (e & 1) ? hi_f(sw_) : lo_f(sw_);
                float o = rbf(own[n][e]);
                float gg = rbf(o * __builtin_amdgcn_rcpf(1.f + __expf(-o)));
                sg[e] = s * gg;
            }
            *(u2*)(tb + r16 * 144 + 32 * n + 8 * g) = (u2){pk_bf16(sg[0], sg[1]), pk_bf16(sg[2], sg[3])};
        }
#pragma unroll
        for (int h = 0; h < 2; ++h) {
            const u4 sgv = *(const u4*)(tb + (8 * h + (lane >> 3)) * 144 + 16 * (lane & 7));
            const u4 uv = h ? uu1 : uu0;
            u4 o;
#pragma unroll
            for (int e = 0; e < 4; ++e) { const unsigned a = uv[e], c = sgv[e]; o[e] = pk_bf16(lo_f(a) + lo_f(c), hi_f(a) + hi_f(c)); }
            *(u4*)(out + uo + (size_t)h * 8 * D_) = o;
        }
    }
}

__device__ __forceinline__ f2 cmul(f2 a, f2 b) {
    f2 t = a * b.xx;
    f2 by = b.yy; by.x = -by.x;
    return __builtin_elementwise_fma(a.yx, by, t);
}
__device__ __forceinline__ f2 zpow(float a2, float wv, float p) {
    float e = __builtin_amdgcn_exp2f(a2 * p);
    float th = __builtin_amdgcn_fractf(wv * p);
    return (f2){e * __builtin_amdgcn_cosf(th), e * __builtin_amdgcn_sinf(th)};
}
struct c2 { f2 a, b; };
__device__ __forceinline__ c2 cmul2(c2 x, c2 y) { c2 o; o.a = cmul(x.a, y.a); o.b = cmul(x.b, y.b); return o; }
__device__ __forceinline__ u2 pkc(c2 v) { return (u2){pk_f16(v.a.x, v.a.y), pk_f16(v.b.x, v.b.y)}; }
__device__ __forceinline__ u2 pkcn(c2 v) { return (u2){pk_f16(v.a.x, -v.a.y), pk_f16(v.b.x, -v.b.y)}; }
#define KS_ROW 528
#define KS_WAVE (16 * KS_ROW)
#define SSM_KS   0
#define SSM_XC   67584
#define SSM_KR   134144
#define SSM_KD   152576
#define SSM_BD   157824
#define SSM_RED  0

__device__ __forceinline__ void ssm_role(const uint16_t* __restrict__ u, const uint16_t* __restrict__ lar, const uint16_t* __restrict__ aim,
                                         const uint16_t* __restrict__ cr, const uint16_t* __restrict__ ci,
                                         const uint16_t* __restrict__ ldt, const uint16_t* __restrict__ dsk,
                                         char* ws, char* lds, int sb) {
    const int tid = threadIdx.x, lane = tid & 63, wave = __builtin_amdgcn_readfirstlane(tid >> 6);
    const int r16 = lane & 15, g = lane >> 4;
    const int c0 = 4 * sb;
    const int ch = wave >> 1;
    u64 tag = 0;
    const __amdgpu_buffer_rsrc_t rs = __builtin_amdgcn_make_buffer_rsrc(ws, 0, WS_BYTES, 0x00020000);
    u2 ucol[4];
    const u2 dsk4 = *(const u2*)(dsk + c0);
    {
        const int c = c0 + ch, n0 = c * N_ + 128 * (wave & 1) + 2 * lane;
        const unsigned wl = *(const unsigned*)(lar + n0), wa = *(const unsigned*)(aim + n0), wr = *(const unsigned*)(cr + n0), wi = *(const unsigned*)(ci + n0);
        const uint16_t wdt = ldt[c];
        if (wave == 7) tag = *(const u64*)(ws + WS_EPOCH) + 1ull;
        const float dt = rbf(expf(bf2f(wdt)));
        const float ar0 = -rbf(expf(lo_f(wl))) * dt, ar1 = -rbf(expf(hi_f(wl))) * dt;
        const float ai0 = lo_f(wa) * dt, ai1 = hi_f(wa) * dt;
        const float Cr0 = lo_f(wr) * dt, Cr1 = hi_f(wr) * dt, Ci0 = lo_f(wi) * dt, Ci1 = hi_f(wi) * dt;
        float bd = row16_sum((fabsf(Cr0) + fabsf(Ci0)) + (fabsf(Cr1) + fabsf(Ci1)));
        bd += __shfl_xor(bd, 16); bd += __shfl_xor(bd, 32);
        int eb = (int)((__float_as_uint(bd) >> 23) & 255u) - 126;
        eb = eb < -100 ? -100 : eb;
        const float S1 = __uint_as_float((unsigned)(127 + 14 - eb) << 23);
        const float S1inv = __uint_as_float((unsigned)(127 + eb - 27) << 23);
        const float a20 = ar0 * 1.44269504088896f, wv0 = ai0 * 0.159154943091895f, a21 = ar1 * 1.44269504088896f, wv1 = ai1 * 0.159154943091895f;
        char* sc = lds + SSM_KS + wave * KS_WAVE;
        char* wp = sc + 8 * lane;
        const char* rp = sc + r16 * KS_ROW + 16 * g;
#define ZP(p) (c2){zpow(a20, wv0, p), zpow(a21, wv1, p)}
        c2 z1 = ZP(1.f), z8 = ZP(8.f), z64 = ZP(64.f), z128 = ZP(128.f), z1024 = ZP(1024.f);
#define WR(e, v) *(u2*)(wp + (e) * KS_ROW) = (v)
        u4 bfr[8];
        {
            c2 cj = {(f2){16384.f, 0.f}, (f2){16384.f, 0.f}};
#pragma unroll
            for (int j = 0; j < 8; ++j) {
                c2 hi = cmul2(cj, z64);
                WR(j, pkc(cj)); WR(8 + j, pkc(hi));
                if (j < 7) cj = cmul2(cj, z8);
            }
#pragma unroll
            for (int k4 = 0; k4 < 8; ++k4) bfr[k4] = *(const u4*)(rp + 64 * k4);
        }
        asm volatile("" :: "v"(bfr[0]) : "memory");
#pragma unroll
        for (int e = 0; e < 4; ++e) ucol[e] = *(const u2*)(u + (size_t)(4 * tid + e) * D_ + c0);
        c2 ze[8];
        ze[1] = z1; ze[2] = cmul2(z1, z1); ze[3] = cmul2(ze[2], z1); ze[4] = cmul2(ze[2], ze[2]);
        ze[5] = cmul2(ze[4], z1); ze[6] = cmul2(ze[4], ze[2]); ze[7] = cmul2(ze[4], ze[3]);
        c2 b = {(f2){Cr0 * S1, Ci0 * S1}, (f2){Cr1 * S1, Ci1 * S1}};
        c2 bh = cmul2(b, z1024);
        f4 acc[3];
#pragma unroll
        for (int i = 0; i < 2; ++i) {
            WR(8 * i, pkcn(b));
#pragma unroll
            for (int e = 1; e < 8; ++e) WR(8 * i + e, pkcn(cmul2(ze[e], b)));
            b = cmul2(b, z128);
        }
        acc[0] = (f4){0, 0, 0, 0};
#pragma unroll
        for (int k4 = 0; k4 < 8; ++k4) acc[0] = mfma_h(*(const u4*)(rp + 64 * k4), bfr[k4], acc[0]);
#pragma unroll
        for (int i = 0; i < 2; ++i) {
            WR(4 * i, pkcn(b));
#pragma unroll
            for (int e = 1; e < 4; ++e) WR(4 * i + e, pkcn(cmul2(ze[2 * e], b)));
            b = cmul2(b, z128);
        }
#pragma unroll
        for (int i = 0; i < 4; ++i) {
            WR(8 + 2 * i, pkcn(b));
            WR(8 + 2 * i + 1, pkcn(cmul2(b, ze[4])));
            if (i < 3) b = cmul2(b, z128);
        }
        acc[1] = (f4){0, 0, 0, 0};
#pragma unroll
        for (int k4 = 0; k4 < 8; ++k4) acc[1] = mfma_h(*(const u4*)(rp + 64 * k4), bfr[k4], acc[1]);
#pragma unroll
        for (int i = 0; i < 9; ++i) {
            WR(i, pkcn(bh));
            if (i < 8) bh = cmul2(bh, z128);
        }
        acc[2] = (f4){0, 0, 0, 0};
#pragma unroll
        for (int k4 = 0; k4 < 8; ++k4) acc[2] = mfma_h(*(const u4*)(rp + 64 * k4), bfr[k4], acc[2]);
        float* pd = (float*)sc;
        *(f4*)(pd + 128 * (g >> 1) + 4 * (g & 1) + 8 * r16) = acc[0] * S1inv;
        if (g < 2) *(f4*)(pd + 256 + 64 * g + 4 * r16) = acc[1] * S1inv;
        else {
            *(f2*)(pd + 384 + 64 * (g - 2) + 2 * r16) = (f2){acc[1][0], acc[1][1]} * S1inv;
            *(f2*)(pd + 384 + 64 * (g - 2) + 32 + 2 * r16) = (f2){acc[1][2], acc[1][3]} * S1inv;
        }
        if (g < 3) {
#pragma unroll
            for (int e = 0; e < 4; ++e) pd[512 + 16 * (4 * g + e) + r16] = acc[2][e] * S1inv;
        }
        if (lane == 0) *(float*)(lds + SSM_BD + 4 * wave) = bd;
    }
    __syncthreads();
    float SKinv[4];
    {
        const float* bdp = (const float*)(lds + SSM_BD);
        float SKs[4];
#pragma unroll
        for (int c = 0; c < 4; ++c) {
            float bb = 2.f * (bdp[2 * c] + bdp[2 * c + 1]);
            int e0 = (int)((__float_as_uint(bb) >> 23) & 255u) - 126; e0 = e0 < -100 ? -100 : e0;
            SKinv[c] = __uint_as_float((unsigned)(127 - 15 + e0) << 23); SKs[c] = __uint_as_float((unsigned)(127 + 15 - e0) << 23);
        }
        const int c = tid >> 7, q = tid & 127;
        const float SK = c == 0 ? SKs[0] : c == 1 ? SKs[1] : c == 2 ? SKs[2] : SKs[3];
#pragma unroll
        for (int rep = 0; rep < 2; ++rep) {
            const int qq = q + 128 * rep;
            if (qq < 164) {
                const char* s0 = lds + SSM_KS + (2 * c) * KS_WAVE + qq * 16;
                f4 v = (*(const f4*)s0 + *(const f4*)(s0 + KS_WAVE)) * SK;
                *(u2*)(lds + SSM_KD + c * 1312 + qq * 8) = (u2){pk_f16(v[0], v[1]), pk_f16(v[2], v[3])};
            }
        }
    }
    if (tid < 128) { *(u2*)(lds + SSM_XC + (tid >> 5) * 16640 + ((tid >> 3) & 3) * 4160 + 8 * (tid & 7)) = (u2){0, 0}; }
    if (wave == 7) wait_flags((const gu64*)(ws + WS_FX) + (sb & (NREP - 1)) * 64 + (lane & 31), true, tag);
    __syncthreads();
    const u4 st = __builtin_amdgcn_raw_buffer_load_b128(rs, WS_ST + tid * 16, 0, 16);
    {
        const int xa_i = tid & 255;
        int idx[8];
#pragma unroll
        for (int j = 0; j < 8; j += 2) {
            int l0 = 2047 - (8 * xa_i + j), l1 = l0 - 1;
            unsigned bits = pk_bf16((float)l0, (float)l1);
            idx[j] = l0 < 128 ? l0 : (int)((bits & 0xffffu) - 0x4280u);
            idx[j + 1] = l1 < 128 ? l1 : (int)((bits >> 16) - 0x4280u);
        }
#pragma unroll
        for (int rep = 0; rep < 2; ++rep) {
            const int c = (tid >> 8) + 2 * rep;
            const uint16_t* kd = (const uint16_t*)(lds + SSM_KD + c * 1312);
            u4 o;
#pragma unroll
            for (int j = 0; j < 4; ++j) o[j] = (unsigned)kd[idx[2 * j]] | ((unsigned)kd[idx[2 * j + 1]] << 16);
            char* kr = lds + SSM_KR + c * 4608;
            *(u4*)(kr + 16 * xa_i) = o;
            if (xa_i < 32) *(u4*)(kr + 4096 + 16 * xa_i) = (u4){0, 0, 0, 0};
        }
    }
    {
        float xv[4][4];
#pragma unroll
        for (int e = 0; e < 4; ++e) {
            const unsigned sw_ = st[e];
            const float mb = lo_f(sw_), rinv = __builtin_amdgcn_rcpf(hi_f(sw_));
#pragma unroll
            for (int h = 0; h < 2; ++h) {
                const unsigned uw = ucol[e][h];
                unsigned a = pk_bf16(lo_f(uw) - mb, hi_f(uw) - mb);
                unsigned r = pk_bf16(lo_f(a) * rinv, hi_f(a) * rinv);
                xv[2 * h][e] = lo_f(r); xv[2 * h + 1][e] = hi_f(r);
            }
        }
#pragma unroll
        for (int cc = 0; cc < 4; ++cc) {
            const unsigned d0 = pk_f16(xv[cc][0], xv[cc][1]), d1 = pk_f16(xv[cc][2], xv[cc][3]);
            const unsigned n0 = __shfl_down(d0, 1), n1 = __shfl_down(d1, 1);
            const unsigned mid = __builtin_amdgcn_alignbyte(d1, d0, 2), e1 = __builtin_amdgcn_alignbyte(n0, d1, 2), e2 = __builtin_amdgcn_alignbyte(n1, n0, 2);
            char* xc = lds + SSM_XC + cc * 16640 + 8 * (tid + 8);
            *(u2*)(xc) = (u2){d0, d1};
            if (lane < 63) {
                *(u2*)(xc + 4160) = (u2){mid, e1};
                *(u2*)(xc + 2 * 4160) = (u2){d1, n0};
                *(u2*)(xc + 3 * 4160) = (u2){e1, e2};
            } else {
                *(unsigned*)(xc + 4160) = mid; *(uint16_t*)(xc + 4160 + 4) = (uint16_t)(d1 >> 16);
                *(unsigned*)(xc + 2 * 4160) = d1;
                *(uint16_t*)(xc + 3 * 4160) = (uint16_t)(d1 >> 16);
            }
            if (lane == 0) {
                *(uint16_t*)(xc + 4160 - 2) = (uint16_t)d0;
                *(unsigned*)(xc + 2 * 4160 - 4) = d0;
                *(uint16_t*)(xc + 3 * 4160 - 6) = (uint16_t)d0; *(unsigned*)(xc + 3 * 4160 - 4) = mid;
            }
        }
    }
    __syncthreads();
    f4 acc[8];
#pragma unroll
    for (int n = 0; n < 8; ++n) acc[n] = (f4){0, 0, 0, 0};
    {
        const int rh = wave & 1;
        const char* xc = lds + SSM_XC + ch * 16640 + ((r16 + 1) & 3) * 4160 + 8 * (10 - 2 * g + ((r16 + 1) >> 2)) + 64 * 4 * rh;
        const char* kr = lds + SSM_KR + ch * 4608 + 16 * (257 - 2 * r16 - g) + 64 * 4 * rh;
#pragma unroll
        for (int rr = 0; rr < 4; ++rr) {
            u4 af[8], bfv[8];
#pragma unroll
            for (int kp = 0; kp < 8; ++kp) {
                const char* p = xc + 64 * (8 * kp + rr);
                u2 a = *(const u2*)p, b = *(const u2*)(p + 8);
                af[kp] = (u4){a.x, a.y, b.x, b.y};
            }
#pragma unroll
            for (int j = 0; j < 8; ++j) bfv[j] = *(const u4*)(kr - 64 * (8 * j - rr));
#pragma unroll
            for (int n = 0; n < 8; ++n)
#pragma unroll
                for (int kp = 0; kp <= n; ++kp) acc[n] = mfma_h(af[kp], bfv[n - kp], acc[n]);
        }
    }
    {
        char* red = lds + SSM_RED;
#pragma unroll
        for (int n = 0; n < 8; ++n) *(f4*)(red + ((wave * 8 + n) * 64 + lane) * 16) = acc[n];
    }
    __syncthreads();
    {
        const char* red = lds + SSM_RED;
        const int t = 256 * wave + 16 * r16 + 4 * g;
        float s[4][4];
#pragma unroll
        for (int c = 0; c < 4; ++c) {
            f4 y = *(const f4*)(red + (((2 * c) * 8 + wave) * 64 + lane) * 16) + *(const f4*)(red + (((2 * c + 1) * 8 + wave) * 64 + lane) * 16);
            const u2 xx = *(const u2*)(lds + SSM_XC + c * 16640 + 2 * (t + 32));
            const unsigned dw_ = dsk4[c >> 1];
            const float ds = (c & 1) ? hi_f(dw_) : lo_f(dw_);
#pragma unroll
            for (int e = 0; e < 4; ++e) {
                const unsigned xw = xx[e >> 1];
                float x = (e & 1) ? hi_h(xw) : lo_h(xw);
                s[c][e] = rbf(y[e] * SKinv[c]) + rbf(x * ds);
            }
        }
        const unsigned soff = WS_S + sb * 16384 + t * 8;
        __builtin_amdgcn_raw_buffer_store_b128((u4){pk_bf16(s[0][0], s[1][0]), pk_bf16(s[2][0], s[3][0]), pk_bf16(s[0][1], s[1][1]), pk_bf16(s[2][1], s[3][1])}, rs, soff, 0, 16);
        __builtin_amdgcn_raw_buffer_store_b128((u4){pk_bf16(s[0][2], s[1][2]), pk_bf16(s[2][2], s[3][2]), pk_bf16(s[0][3], s[1][3]), pk_bf16(s[2][3], s[3][3])}, rs, soff + 16, 0, 16);
    }
    WAIT_VM(0);
    __syncthreads();
    if (tid >= 448 && tid < 448 + NREP) __hip_atomic_store((gu64*)(ws + WS_FS) + (tid - 448) * 128 + sb, tag, RLX_AGENT);
}

__global__ __launch_bounds__(512, 2) void fused_kernel(const uint16_t* __restrict__ u, const uint16_t* __restrict__ lar, const uint16_t* __restrict__ aim,
                                                       const uint16_t* __restrict__ cr, const uint16_t* __restrict__ ci,
                                                       const uint16_t* __restrict__ ldt, const uint16_t* __restrict__ dsk,
                                                       const uint16_t* __restrict__ w1, const uint16_t* __restrict__ w2,
                                                       char* ws, uint16_t* __restrict__ out) {
    extern __shared__ __attribute__((aligned(16))) char lds[];
    const int b = blockIdx.x;
    if (b < NFFN) ffn_role(u, w1, w2, ws, out, lds, b);
    else if (b < NFFN + NSSM) ssm_role(u, lar, aim, cr, ci, ldt, dsk, ws, lds, b - NFFN);
    else {
        const int p = b - NFFN - NSSM, lane = threadIdx.x & 63, wave = __builtin_amdgcn_readfirstlane(threadIdx.x >> 6);
        const __amdgpu_buffer_rsrc_t rs = __builtin_amdgcn_make_buffer_rsrc(ws, 0, WS_BYTES, 0x00020000);
        const u64 tag = *(const u64*)(ws + WS_EPOCH) + 1ull;
        const u4 ones = {0xffffffffu, 0xffffffffu, 0xffffffffu, 0xffffffffu};
        const int blk = 8 * p + wave, ks = blk & 3;
        const unsigned pb = WS_PART + (unsigned)(blk * 56) * 1024 + lane * 16;
        for (int i = 0; i < 56; ++i) if (((i >> 1) & 3) != ks) __builtin_amdgcn_raw_buffer_store_b128(ones, rs, pb + i * 1024, 0, 16);
        __builtin_amdgcn_raw_buffer_store_b128(ones, rs, WS_PSC + blk * 2048 + lane * 16, 0, 16);
        __builtin_amdgcn_raw_buffer_store_b128(ones, rs, WS_PSC + blk * 2048 + 1024 + lane * 16, 0, 16);
        WAIT_VM(0);
        __syncthreads();
        if (threadIdx.x < 2) __hip_atomic_store((gu64*)(ws + WS_FP) + (2 * p + threadIdx.x) * 16 + 4, tag, RLX_AGENT);
    }
}

extern "C" void kernel_launch(void* const* d_in, const int* in_sizes, int n_in,
                              void* d_out, int out_size, void* d_ws, size_t ws_size,
                              hipStream_t stream) {
    (void)hipFuncSetAttribute((const void*)fused_kernel, hipFuncAttributeMaxDynamicSharedMemorySize, LDS_BYTES);
    fused_kernel<<<NFFN + NSSM + 16, 512, LDS_BYTES, stream>>>((const uint16_t*)d_in[0], (const uint16_t*)d_in[1], (const uint16_t*)d_in[2], (const uint16_t*)d_in[3],
                                                  (const uint16_t*)d_in[4], (const uint16_t*)d_in[5], (const uint16_t*)d_in[6], (const uint16_t*)d_in[8],
                                                  (const uint16_t*)d_in[9], (char*)d_ws, (uint16_t*)d_out);
}
```

```cpp
#include <hip/hip_runtime.h>
#include <stdint.h>

#define L_ 2048
#define D_ 448
#define N_ 256
#define F_ 1792

typedef __attribute__((ext_vector_type(8))) __bf16 bf8;
typedef __attribute__((ext_vector_type(8))) _Float16 h8;
typedef __attribute__((ext_vector_type(4))) short s4v;
typedef __attribute__((ext_vector_type(4))) float f4;
typedef __attribute__((ext_vector_type(2))) float f2;
typedef __attribute__((ext_vector_type(4))) unsigned u4;
typedef __attribute__((ext_vector_type(2))) unsigned u2;
typedef __attribute__((ext_vector_type(2))) __bf16 bf2v;
typedef __attribute__((ext_vector_type(2))) _Float16 h2v;
typedef unsigned long long u64;
#define LAS __attribute__((address_space(3)))
#define GAS __attribute__((address_space(1)))
typedef GAS u64 gu64;
#define RLX_AGENT __ATOMIC_RELAXED, __HIP_MEMORY_SCOPE_AGENT

__device__ __forceinline__ float bf2f(uint16_t v) { return __uint_as_float(((uint32_t)v) << 16); }
__device__ __forceinline__ unsigned pk_bf16(float a, float b) { bf2v r; r.x = (__bf16)a; r.y = (__bf16)b; return __builtin_bit_cast(unsigned, r); }
__device__ __forceinline__ unsigned pk_f16(float a, float b) { h2v r; r.x = (_Float16)a; r.y = (_Float16)b; return __builtin_bit_cast(unsigned, r); }
__device__ __forceinline__ float rbf(float f) { return __uint_as_float(pk_bf16(f, 0.f) << 16); }
__device__ __forceinline__ float lo_f(unsigned w) { return __uint_as_float(w << 16); }
__device__ __forceinline__ float hi_f(unsigned w) { return __uint_as_float(w & 0xffff0000u); }
__device__ __forceinline__ float lo_h(unsigned w) { return (float)__builtin_bit_cast(h2v, w).x; }
__device__ __forceinline__ float hi_h(unsigned w) { return (float)__builtin_bit_cast(h2v, w).y; }
__device__ __forceinline__ f4 mfma_bf(u4 a, u4 b, f4 c) { return __builtin_amdgcn_mfma_f32_16x16x32_bf16(__builtin_bit_cast(bf8, a), __builtin_bit_cast(bf8, b), c, 0, 0, 0); }
__device__ __forceinline__ f4 mfma_h(u4 a, u4 b, f4 c) { return __builtin_amdgcn_mfma_f32_16x16x32_f16(__builtin_bit_cast(h8, a), __builtin_bit_cast(h8, b), c, 0, 0, 0); }
template <int CTRL> __device__ __forceinline__ float dpp_f(float v) { return __int_as_float(__builtin_amdgcn_update_dpp(0, __float_as_int(v), CTRL, 0xf, 0xf, false)); }
__device__ __forceinline__ float row16_sum(float v) { v += dpp_f<0x128>(v); v += dpp_f<0x124>(v); v += dpp_f<0x122>(v); v += dpp_f<0x121>(v); return v; }
__device__ __forceinline__ float row16_max(float v) { v = fmaxf(v, dpp_f<0x128>(v)); v = fmaxf(v, dpp_f<0x124>(v)); v = fmaxf(v, dpp_f<0x122>(v)); v = fmaxf(v, dpp_f<0x121>(v)); return v; }
__device__ __forceinline__ u2 tr1(const char* p) { return __builtin_bit_cast(u2, __builtin_amdgcn_ds_read_tr16_b64_v4i16((LAS s4v*)p)); }
__device__ __forceinline__ void dma_pair(unsigned dst, unsigned va, unsigned vb, const void* sbase) {
    asm volatile("s_mov_b32 m0, %0\n\ts_nop 0\n\tglobal_load_lds_dwordx4 %1, %3\n\tglobal_load_lds_dwordx4 %2, %3 offset:1024" :: "s"(dst), "v"(va), "v"(vb), "s"(sbase) : "memory");
}
#define WAIT_VM(n) asm volatile("s_waitcnt vmcnt(" #n ")" ::: "memory")
#define WAIT_LGKM0() asm volatile("s_waitcnt lgkmcnt(0)" ::: "memory")
#define RAW_BAR() do { WAIT_LGKM0(); __builtin_amdgcn_s_barrier(); asm volatile("" ::: "memory"); } while (0)

#define SLEEPN 4

#define NFFN 128
#define NSSM 112
#define WS_EPOCH 0
#define NREP 16
#define WS_FX    65536
#define WS_ST    32768
#define WS_FS    131072
#define WS_FP    196608
#define WS_PSC   262144
#define WS_S     (3u << 20)
#define WS_PART  (5u << 20)
#define WS_BYTES (16u << 20)
#define LDS_BYTES 160000
#define SPIN_CAP (1u << 14)

__device__ __forceinline__ void wait_flags(const gu64* p, bool active, u64 tag) {
    for (unsigned spins = 0; spins < SPIN_CAP; ++spins) {
        u64 v = active ? __hip_atomic_load(p, RLX_AGENT) : tag;
        if (__all(v == tag)) break;
        __builtin_amdgcn_s_sleep(SLEEPN);
    }
    __builtin_amdgcn_fence(__ATOMIC_ACQUIRE, "wavefront");
}

#define XN_BYTES 57344
#define RING_OFF XN_BYTES
#define RSL 7
#define RING_W (RSL * 2048)
#define ST_OFF (RING_OFF + 7 * RING_W)
__device__ __forceinline__ void ffn_role(const uint16_t* __restrict__ u, const uint16_t* __restrict__ w1, const uint16_t* __restrict__ w2,
                                         char* ws, uint16_t* __restrict__ out, char* lds, int b) {
    const int tid = threadIdx.x, lane = tid & 63, wave = __builtin_amdgcn_readfirstlane(tid >> 6);
    const int r16 = lane & 15, g = lane >> 4;
    const int xcd = b & 7, ks = xcd & 3, rt = 16 * (xcd >> 2) + (b >> 3);
    const int rot = (b >> 3) % 14;
    const __amdgpu_buffer_rsrc_t rs = __builtin_amdgcn_make_buffer_rsrc(ws, 0, WS_BYTES, 0x00020000);
    u4 ur[2][4];
    {
        const u4* up = (const u4*)(u + (size_t)(64 * rt + 8 * wave + g) * D_) + (r16 < 14 ? r16 : 13);
        const u4* up2 = up + 4 * 56;
        asm volatile("global_load_dwordx4 %0, %8, off\n\tglobal_load_dwordx4 %1, %8, off offset:224\n\tglobal_load_dwordx4 %2, %8, off offset:448\n\tglobal_load_dwordx4 %3, %8, off offset:672\n\t"
                     "global_load_dwordx4 %4, %9, off\n\tglobal_load_dwordx4 %5, %9, off offset:224\n\tglobal_load_dwordx4 %6, %9, off offset:448\n\tglobal_load_dwordx4 %7, %9, off offset:672"
                     : "=&v"(ur[0][0]), "=&v"(ur[0][1]), "=&v"(ur[0][2]), "=&v"(ur[0][3]), "=&v"(ur[1][0]), "=&v"(ur[1][1]), "=&v"(ur[1][2]), "=&v"(ur[1][3])
                     : "v"(up), "v"(up2) : "memory");
    }
    const unsigned ldsbase = (unsigned)(size_t)(LAS char*)lds;
    const unsigned ring = ldsbase + RING_OFF + wave * RING_W;
    const int rho = lane >> 3;
    const unsigned srcc = (unsigned)(((lane & 7) ^ (((rho >> 1) & 3) << 1)) * 16);
    const int krow = 8 * (rho >> 2) + (rho & 3);
    const unsigned v1a = krow * (F_ * 2) + srcc, v1b = v1a + 16 * F_ * 2 - 1024;
    const unsigned v2a = krow * (D_ * 2) + srcc, v2b = v2a + 16 * D_ * 2 - 1024;
    const char* w1b = (const char*)(w1 + 448 * ks + 64 * wave);
    const char* w2b = (const char*)(w2 + (size_t)448 * ks * D_ + 64 * wave);
#define KROT(i) ((i) + rot >= 14 ? (i) + rot - 14 : (i) + rot)
#define ISSUE(s) do { if ((s) < 28) dma_pair(ring + ((s) % RSL) * 2048, v1a, v1b, w1b + (size_t)KROT((s) >> 1) * (32 * F_ * 2) + ((s) & 1) * (4 * F_ * 2)); \
                      else if ((s) < 56) dma_pair(ring + ((s) % RSL) * 2048, v2a, v2b, w2b + (size_t)KROT(((s) - 28) >> 1) * (32 * D_ * 2) + ((s) & 1) * (4 * D_ * 2)); } while (0)
    u64 tag = 0;
    if (wave < 7) { ISSUE(0); ISSUE(1); ISSUE(2); ISSUE(3); }
    else WAIT_VM(0);
    asm volatile("s_waitcnt vmcnt(8)" : "+v"(ur[0][0]), "+v"(ur[0][1]), "+v"(ur[0][2]), "+v"(ur[0][3]), "+v"(ur[1][0]), "+v"(ur[1][1]), "+v"(ur[1][2]), "+v"(ur[1][3]) :: "memory");
    if (wave == 7) { __builtin_amdgcn_s_setprio(3); tag = *(const u64*)(ws + WS_EPOCH) + 1ull; }
#pragma unroll
    for (int p = 0; p < 2; ++p) {
        const bool act = r16 < 14;
        const bf2v one2 = {(__bf16)1.0f, (__bf16)1.0f};
        float s = 0.f;
#pragma unroll
        for (int i = 0; i < 4; ++i)
#pragma unroll
            for (int e = 0; e < 4; ++e) { const unsigned w_ = ur[p][i][e]; s = __builtin_amdgcn_fdot2_f32_bf16(__builtin_bit_cast(bf2v, w_), one2, s, false); }
        s = row16_sum(act ? s : 0.f);
        const float mean = s * (1.0f / D_);
        const float mb = rbf(mean);
        unsigned av[4][4];
        f2 q2 = {0.f, 0.f};
#pragma unroll
        for (int i = 0; i < 4; ++i)
#pragma unroll
            for (int e = 0; e < 4; ++e) {
                f2 d = (f2){lo_f(ur[p][i][e]), hi_f(ur[p][i][e])} - (f2){mb, mb};
                q2 = __builtin_elementwise_fma(d, d, q2);
                av[i][e] = pk_bf16(d.x, d.y);
            }
#pragma unroll
        for (int i = 0; i < 4; ++i) asm volatile("" : "+v"(av[i][0]), "+v"(av[i][1]), "+v"(av[i][2]), "+v"(av[i][3]));
        const float q = row16_sum(act ? q2.x + q2.y : 0.f);
        const float dm = mean - mb;
        const float var = fmaxf(fmaf(-dm, dm, q * (1.0f / D_)), 0.f);
        const float sd = rbf(__builtin_amdgcn_sqrtf(rbf(rbf(var) + 1e-5f)));
        const float rinv = __builtin_amdgcn_rcpf(sd);
        const int lrow = 8 * wave + 4 * p + g;
        if (r16 == 0) *(unsigned*)(lds + ST_OFF + 4 * lrow) = (__float_as_uint(mb) >> 16) | (__float_as_uint(sd) & 0xffff0000u);
        char* xr = lds + lrow * 896;
        const int sw = (lrow >> 1) & 7;
#pragma unroll
        for (int i = 0; i < 4; ++i) {
            u4 o;
#pragma unroll
            for (int e = 0; e < 4; ++e) {
                f2 r = (f2){lo_f(av[i][e]), hi_f(av[i][e])} * (f2){rinv, rinv};
                o[e] = pk_bf16(r.x, r.y);
            }
            if (act) *(u4*)(xr + 16 * ((r16 + 14 * i) ^ sw)) = o;
        }
    }
    if (wave < 7) { ISSUE(4); ISSUE(5); ISSUE(6); }
    if (tid == 0) *(unsigned*)(lds + ST_OFF + 256) = 0u;
    RAW_BAR();
    const int q4 = r16 >> 2, p4 = lane & 3;
    if (wave == 7) {
        if (ks == 0) {
            __builtin_amdgcn_raw_buffer_store_b32(*(const unsigned*)(lds + ST_OFF + 4 * lane), rs, WS_ST + (64 * rt + lane) * 4, 0, 16);
            WAIT_VM(0);
            if (lane < NREP) __hip_atomic_store((gu64*)(ws + WS_FX) + lane * 64 + rt, tag, RLX_AGENT);
        }
        {
            const u4 ones = {0xffffffffu, 0xffffffffu, 0xffffffffu, 0xffffffffu};
            const unsigned pb = WS_PART + (unsigned)((rt * 4 + ks) * 56) * 1024 + lane * 16;
            for (int i = 0; i < 56; ++i) if (((i >> 1) & 3) != ks) __builtin_amdgcn_raw_buffer_store_b128(ones, rs, pb + i * 1024, 0, 16);
            __builtin_amdgcn_raw_buffer_store_b128(ones, rs, WS_PSC + (rt * 4 + ks) * 2048 + lane * 16, 0, 16);
            __builtin_amdgcn_raw_buffer_store_b128(ones, rs, WS_PSC + (rt * 4 + ks) * 2048 + 1024 + lane * 16, 0, 16);
            WAIT_VM(0);
            if (lane == 0) __hip_atomic_store((gu64*)(ws + WS_FP) + rt * 16 + ks, tag, RLX_AGENT);
        }
        __builtin_amdgcn_s_setprio(0);
        RAW_BAR();
        RAW_BAR();
        wait_flags((const gu64*)(ws + WS_FS) + (rt & (NREP - 1)) * 128 + lane, true, tag);
        {
            const gu64* p = lane < 48 ? (const gu64*)(ws + WS_FS) + (rt & (NREP - 1)) * 128 + 64 + lane : (const gu64*)(ws + WS_FP) + rt * 16 + (lane & 3);
            wait_flags(p, lane < 52, tag);
        }
        if (lane == 0) __hip_atomic_store((LAS unsigned*)((LAS char*)lds + ST_OFF + 256), 1u, __ATOMIC_RELAXED, __HIP_MEMORY_SCOPE_WORKGROUP);
        if (b == 0) {
#pragma unroll
            for (int i = 0; i < 2; ++i) wait_flags((const gu64*)(ws + WS_FP) + (16 * i + (lane >> 2)) * 16 + (lane & 3), true, tag);
            if (lane == 0) __hip_atomic_store((gu64*)(ws + WS_EPOCH), tag, RLX_AGENT);
        }
        return;
    }
    if (wave >= 4) __builtin_amdgcn_s_setprio(1);
    const int rr = 4 * g + q4;
    const int wtr = rr * 128 + (((rr >> 1) & 3) << 5) + 8 * p4;
    const char* rpb = lds + RING_OFF + wave * RING_W;
    const char* rp0 = rpb + wtr; const char* rp1 = rpb + (wtr ^ 32); const char* rp2 = rpb + (wtr ^ 64); const char* rp3 = rpb + (wtr ^ 96);
    const int asw = (r16 >> 1) & 7;
    const unsigned ae = (unsigned)((g ^ asw) * 16);
    const char* a0p = lds + r16 * 896;
    f4 acc[4][4];
#pragma unroll
    for (int m = 0; m < 4; ++m)
#pragma unroll
        for (int n = 0; n < 4; ++n) acc[m][n] = (f4){0, 0, 0, 0};
    u4 af[2][4]; u2 x[2][2][4];
#define VMW(n) do { if ((n) >= 10) WAIT_VM(10); else if ((n) >= 8) WAIT_VM(8); else if ((n) >= 6) WAIT_VM(6); else if ((n) >= 4) WAIT_VM(4); else if ((n) >= 2) WAIT_VM(2); else WAIT_VM(0); } while (0)
#define LOADW(j, bsel, issued) do { VMW(2 * ((issued) - (2 * (j) + 1))); \
        _Pragma("unroll") for (int hf = 0; hf < 2; ++hf) { const int so = ((2 * (j) + hf) % RSL) * 2048; \
            x[bsel][hf][0] = tr1(rp0 + so); x[bsel][hf][1] = tr1(rp1 + so); x[bsel][hf][2] = tr1(rp2 + so); x[bsel][hf][3] = tr1(rp3 + so); } } while (0)
#define LOADA(it, bsel) do { const char* ap = a0p + (ae ^ (unsigned)(KROT(it) << 6)); \
        _Pragma("unroll") for (int m = 0; m < 4; ++m) af[bsel][m] = *(const u4*)(ap + m * 16 * 896); } while (0)
#define REFILL(j) do { WAIT_LGKM0(); ISSUE(2 * (j) + RSL); ISSUE(2 * (j) + 1 + RSL); } while (0)
#define ISSUED(j) ((2 * (j) + 1 + RSL) < 55 ? (2 * (j) + 1 + RSL) : 55)
    LOADW(0, 0, RSL - 1); LOADA(0, 0);
    REFILL(0);
#pragma unroll
    for (int it = 0; it < 14; ++it) {
        const int cur = it & 1, nxt = cur ^ 1;
        LOADW(it + 1, nxt, ISSUED(it));
        if (it < 13) LOADA(it + 1, nxt);
        __builtin_amdgcn_sched_barrier(0);
#pragma unroll
        for (int n = 0; n < 4; ++n) {
            const u4 wf = {x[cur][0][n].x, x[cur][0][n].y, x[cur][1][n].x, x[cur][1][n].y};
#pragma unroll
            for (int m = 0; m < 4; ++m) acc[m][n] = mfma_bf(wf, af[cur][m], acc[m][n]);
            __builtin_amdgcn_sched_barrier(0);
            if (n == 1) { WAIT_LGKM0(); ISSUE(2 * (it + 1) + RSL); }
            if (n == 2) { ISSUE(2 * (it + 1) + 1 + RSL); }
            __builtin_amdgcn_sched_barrier(0);
        }
    }
    {
        u2 hp[4][4];
#pragma unroll
        for (int m = 0; m < 4; ++m)
#pragma unroll
            for (int n = 0; n < 4; ++n) {
                typedef __attribute__((ext_vector_type(2))) short s2v;
                const unsigned p0 = pk_bf16(acc[m][n][0], acc[m][n][1]), p1 = pk_bf16(acc[m][n][2], acc[m][n][3]);
                hp[m][n] = (u2){__builtin_bit_cast(unsigned, __builtin_elementwise_max(__builtin_bit_cast(s2v, p0), (s2v){0, 0})), __builtin_bit_cast(unsigned, __builtin_elementwise_max(__builtin_bit_cast(s2v, p1), (s2v){0, 0}))};
                acc[m][n] = (f4){0, 0, 0, 0};
            }
        __builtin_amdgcn_sched_barrier(0);
        RAW_BAR();
#pragma unroll
        for (int m = 0; m < 4; ++m)
#pragma unroll
            for (int n = 0; n < 4; ++n)
                *(u2*)(lds + (16 * m + r16) * 896 + 16 * ((8 * wave + 2 * n + (g >> 1)) ^ asw) + 8 * (g & 1)) = hp[m][n];
    }
    RAW_BAR();
    LOADA(0, 0);
#pragma unroll
    for (int it = 0; it < 14; ++it) {
        const int cur = it & 1, nxt = cur ^ 1;
        if (it < 13) { LOADW(14 + it + 1, nxt, ISSUED(14 + it)); LOADA(it + 1, nxt); }
        __builtin_amdgcn_sched_barrier(0);
#pragma unroll
        for (int n = 0; n < 4; ++n) {
            const u4 wf = {x[cur][0][n].x, x[cur][0][n].y, x[cur][1][n].x, x[cur][1][n].y};
#pragma unroll
            for (int m = 0; m < 4; ++m) acc[m][n] = mfma_bf(wf, af[cur][m], acc[m][n]);
            __builtin_amdgcn_sched_barrier(0);
            if (n == 1 && 2 * (14 + it + 1) + RSL < 56) { WAIT_LGKM0(); ISSUE(2 * (14 + it + 1) + RSL); }
            if (n == 2 && 2 * (14 + it + 1) + RSL < 56) { ISSUE(2 * (14 + it + 1) + 1 + RSL); }
            __builtin_amdgcn_sched_barrier(0);
        }
    }
    {
        float mx = 0.f;
#pragma unroll
        for (int m = 0; m < 4; ++m)
#pragma unroll
            for (int n = 0; n < 4; ++n)
#pragma unroll
                for (int e = 0; e < 4; ++e) mx = fmaxf(mx, fabsf(acc[m][n][e]));
        int e2 = (int)((__float_as_uint(mx) >> 23) & 255u) - 126;
        e2 = e2 < -100 ? -100 : e2;
        const float sc = __uint_as_float((unsigned)(127 + 15 - e2) << 23);
        const float scinv = __uint_as_float((unsigned)(127 - 15 + e2) << 23);
        const unsigned pbase = WS_PART + (unsigned)(((rt * 4 + ks) * 7 + wave) * 8) * 1024 + lane * 16;
#pragma unroll
        for (int m = 0; m < 4; ++m) {
            if (m != ks) {
#pragma unroll
                for (int np = 0; np < 2; ++np) {
                    u4 o = {pk_f16(acc[m][2 * np][0] * sc, acc[m][2 * np][1] * sc), pk_f16(acc[m][2 * np][2] * sc, acc[m][2 * np][3] * sc),
                            pk_f16(acc[m][2 * np + 1][0] * sc, acc[m][2 * np + 1][1] * sc), pk_f16(acc[m][2 * np + 1][2] * sc, acc[m][2 * np + 1][3] * sc)};
#pragma unroll
                    for (int e = 0; e < 4; ++e) o[e] = o[e] < 0xfffffffeu ? o[e] : 0xfffffffeu;
                    __builtin_amdgcn_raw_buffer_store_b128(o, rs, pbase + (2 * m + np) * 1024, 0, 16);
                }
            }
        }
        __builtin_amdgcn_raw_buffer_store_b32(__float_as_uint(scinv), rs, WS_PSC + (((rt * 4 + ks) * 8 + wave) * 64 + lane) * 4, 0, 16);
    }
    f4 own[4];
#pragma unroll
    for (int n = 0; n < 4; ++n) own[n] = ks == 0 ? acc[0][n] : ks == 1 ? acc[1][n] : ks == 2 ? acc[2][n] : acc[3][n];
    const int trow = 64 * rt + 16 * ks;
    const size_t uo = (size_t)(trow + (lane >> 3)) * D_ + 64 * wave + 8 * (lane & 7);
    const u4 uu0 = *(const u4*)(u + uo), uu1 = *(const u4*)(u + uo + 8 * D_);
    for (unsigned spins = 0; spins < (1u << 20); ++spins) {
        if (__hip_atomic_load((LAS unsigned*)((LAS char*)lds + ST_OFF + 256), __ATOMIC_RELAXED, __HIP_MEMORY_SCOPE_WORKGROUP)) break;
        __builtin_amdgcn_s_sleep(2);
    }
    asm volatile("" ::: "memory");
    {
        u2 sv[4];
#pragma unroll
        for (int n = 0; n < 4; ++n) sv[n] = __builtin_amdgcn_raw_buffer_load_b64(rs, WS_S + (16 * wave + 4 * n + g) * 16384 + (trow + r16) * 8, 0, 16);
        u4 pv[3][2]; float psc[3];
        for (unsigned spins = 0; spins < SPIN_CAP; ++spins) {
            unsigned mxv = 0;
#pragma unroll
            for (int j = 0; j < 3; ++j) {
                const int kp = (ks + 1 + j) & 3;
#pragma unroll
                for (int np = 0; np < 2; ++np)
                    pv[j][np] = __builtin_amdgcn_raw_buffer_load_b128(rs, WS_PART + (unsigned)((((rt * 4 + kp) * 7 + wave) * 8) + 2 * ks + np) * 1024 + lane * 16, 0, 16);
                psc[j] = __uint_as_float(__builtin_amdgcn_raw_buffer_load_b32(rs, WS_PSC + (((rt * 4 + kp) * 8 + wave) * 64 + lane) * 4, 0, 16));
            }
#pragma unroll
            for (int j = 0; j < 3; ++j) {
#pragma unroll
                for (int np = 0; np < 2; ++np)
#pragma unroll
                    for (int e = 0; e < 4; ++e) { const unsigned w_ = pv[j][np][e]; mxv = mxv > w_ ? mxv : w_; }
                const unsigned w_ = __float_as_uint(psc[j]); mxv = mxv > w_ ? mxv : w_;
            }
            if (!__any(mxv == 0xffffffffu)) break;
            __builtin_amdgcn_s_sleep(1);
            asm volatile("" ::: "memory");
        }
#pragma unroll
        for (int j = 0; j < 3; ++j)
#pragma unroll
            for (int n = 0; n < 4; ++n) {
                const unsigned w0 = pv[j][n >> 1][2 * (n & 1)], w1_ = pv[j][n >> 1][2 * (n & 1) + 1];
                own[n][0] = fmaf(lo_h(w0), psc[j], own[n][0]); own[n][1] = fmaf(hi_h(w0), psc[j], own[n][1]);
                own[n][2] = fmaf(lo_h(w1_), psc[j], own[n][2]); own[n][3] = fmaf(hi_h(w1_), psc[j], own[n][3]);
            }
        char* tb = lds + RING_OFF + wave * RING_W;
#pragma unroll
        for (int n = 0; n < 4; ++n) {
            float sg[4];
#pragma unroll
            for (int e = 0; e < 4; ++e) {
                const unsigned sw_ = sv[n][e >> 1];
                float s = (e & 1) ? hi_f(sw_) : lo_f(sw_);
                float o = rbf(own[n][e]);
                float gg = rbf(o * __builtin_amdgcn_rcpf(1.f + __expf(-o)));
                sg[e] = s * gg;
            }
            *(u2*)(tb + r16 * 144 + 32 * n + 8 * g) = (u2){pk_bf16(sg[0], sg[1]), pk_bf16(sg[2], sg[3])};
        }
#pragma unroll
        for (int h = 0; h < 2; ++h) {
            const u4 sgv = *(const u4*)(tb + (8 * h + (lane >> 3)) * 144 + 16 * (lane & 7));
            const u4 uv = h ? uu1 : uu0;
            u4 o;
#pragma unroll
            for (int e = 0; e < 4; ++e) { const unsigned a = uv[e], c = sgv[e]; o[e] = pk_bf16(lo_f(a) + lo_f(c), hi_f(a) + hi_f(c)); }
            *(u4*)(out + uo + (size_t)h * 8 * D_) = o;
        }
    }
}

__device__ __forceinline__ f2 cmul(f2 a, f2 b) {
    f2 t = a * b.xx;
    f2 by = b.yy; by.x = -by.x;
    return __builtin_elementwise_fma(a.yx, by, t);
}
__device__ __forceinline__ f2 zpow(float a2, float wv, float p) {
    float e = __builtin_amdgcn_exp2f(a2 * p);
    float th = __builtin_amdgcn_fractf(wv * p);
    return (f2){e * __builtin_amdgcn_cosf(th), e * __builtin_amdgcn_sinf(th)};
}
struct c2 { f2 a, b; };
__device__ __forceinline__ c2 cmul2(c2 x, c2 y) { c2 o; o.a = cmul(x.a, y.a); o.b = cmul(x.b, y.b); return o; }
__device__ __forceinline__ u2 pkc(c2 v) { return (u2){pk_f16(v.a.x, v.a.y), pk_f16(v.b.x, v.b.y)}; }
__device__ __forceinline__ u2 pkcn(c2 v) { return (u2){pk_f16(v.a.x, -v.a.y), pk_f16(v.b.x, -v.b.y)}; }
#define KS_ROW 528
#define KS_WAVE (16 * KS_ROW)
#define SSM_KS   0
#define SSM_XC   67584
#define SSM_KR   134144
#define SSM_KD   152576
#define SSM_BD   157824
#define SSM_RED  0

__device__ __forceinline__ void ssm_role(const uint16_t* __restrict__ u, const uint16_t* __restrict__ lar, const uint16_t* __restrict__ aim,
                                         const uint16_t* __restrict__ cr, const uint16_t* __restrict__ ci,
                                         const uint16_t* __restrict__ ldt, const uint16_t* __restrict__ dsk,
                                         char* ws, char* lds, int sb) {
    const int tid = threadIdx.x, lane = tid & 63, wave = __builtin_amdgcn_readfirstlane(tid >> 6);
    const int r16 = lane & 15, g = lane >> 4;
    const int c0 = 4 * sb;
    const int ch = wave >> 1;
    u64 tag = 0;
    const __amdgpu_buffer_rsrc_t rs = __builtin_amdgcn_make_buffer_rsrc(ws, 0, WS_BYTES, 0x00020000);
    u2 ucol[4];
    const u2 dsk4 = *(const u2*)(dsk + c0);
    {
        const int c = c0 + ch, n0 = c * N_ + 128 * (wave & 1) + 2 * lane;
        const unsigned wl = *(const unsigned*)(lar + n0), wa = *(const unsigned*)(aim + n0), wr = *(const unsigned*)(cr + n0), wi = *(const unsigned*)(ci + n0);
        const uint16_t wdt = ldt[c];
        if (wave == 7) tag = *(const u64*)(ws + WS_EPOCH) + 1ull;
        const float dt = rbf(expf(bf2f(wdt)));
        const float ar0 = -rbf(expf(lo_f(wl))) * dt, ar1 = -rbf(expf(hi_f(wl))) * dt;
        const float ai0 = lo_f(wa) * dt, ai1 = hi_f(wa) * dt;
        const float Cr0 = lo_f(wr) * dt, Cr1 = hi_f(wr) * dt, Ci0 = lo_f(wi) * dt, Ci1 = hi_f(wi) * dt;
        float bd = row16_sum((fabsf(Cr0) + fabsf(Ci0)) + (fabsf(Cr1) + fabsf(Ci1)));
        bd += __shfl_xor(bd, 16); bd += __shfl_xor(bd, 32);
        int eb = (int)((__float_as_uint(bd) >> 23) & 255u) - 126;
        eb = eb < -100 ? -100 : eb;
        const float S1 = __uint_as_float((unsigned)(127 + 14 - eb) << 23);
        const float S1inv = __uint_as_float((unsigned)(127 + eb - 27) << 23);
        const float a20 = ar0 * 1.44269504088896f, wv0 = ai0 * 0.159154943091895f, a21 = ar1 * 1.44269504088896f, wv1 = ai1 * 0.159154943091895f;
        char* sc = lds + SSM_KS + wave * KS_WAVE;
        char* wp = sc + 8 * lane;
        const char* rp = sc + r16 * KS_ROW + 16 * g;
#define ZP(p) (c2){zpow(a20, wv0, p), zpow(a21, wv1, p)}
        c2 z1 = ZP(1.f), z8 = ZP(8.f), z64 = ZP(64.f), z128 = ZP(128.f), z1024 = ZP(1024.f);
#define WR(e, v) *(u2*)(wp + (e) * KS_ROW) = (v)
        u4 bfr[8];
        {
            c2 cj = {(f2){16384.f, 0.f}, (f2){16384.f, 0.f}};
#pragma unroll
            for (int j = 0; j < 8; ++j) {
                c2 hi = cmul2(cj, z64);
                WR(j, pkc(cj)); WR(8 + j, pkc(hi));
                if (j < 7) cj = cmul2(cj, z8);
            }
#pragma unroll
            for (int k4 = 0; k4 < 8; ++k4) bfr[k4] = *(const u4*)(rp + 64 * k4);
        }
        asm volatile("" :: "v"(bfr[0]) : "memory");
#pragma unroll
        for (int e = 0; e < 4; ++e) ucol[e] = *(const u2*)(u + (size_t)(4 * tid + e) * D_ + c0);
        c2 ze[8];
        ze[1] = z1; ze[2] = cmul2(z1, z1); ze[3] = cmul2(ze[2], z1); ze[4] = cmul2(ze[2], ze[2]);
        ze[5] = cmul2(ze[4], z1); ze[6] = cmul2(ze[4], ze[2]); ze[7] = cmul2(ze[4], ze[3]);
        c2 b = {(f2){Cr0 * S1, Ci0 * S1}, (f2){Cr1 * S1, Ci1 * S1}};
        c2 bh = cmul2(b, z1024);
        f4 acc[3];
#pragma unroll
        for (int i = 0; i < 2; ++i) {
            WR(8 * i, pkcn(b));
#pragma unroll
            for (int e = 1; e < 8; ++e) WR(8 * i + e, pkcn(cmul2(ze[e], b)));
            b = cmul2(b, z128);
        }
        acc[0] = (f4){0, 0, 0, 0};
#pragma unroll
        for (int k4 = 0; k4 < 8; ++k4) acc[0] = mfma_h(*(const u4*)(rp + 64 * k4), bfr[k4], acc[0]);
#pragma unroll
        for (int i = 0; i < 2; ++i) {
            WR(4 * i, pkcn(b));
#pragma unroll
            for (int e = 1; e < 4; ++e) WR(4 * i + e, pkcn(cmul2(ze[2 * e], b)));
            b = cmul2(b, z128);
        }
#pragma unroll
        for (int i = 0; i < 4; ++i) {
            WR(8 + 2 * i, pkcn(b));
            WR(8 + 2 * i + 1, pkcn(cmul2(b, ze[4])));
            if (i < 3) b = cmul2(b, z128);
        }
        acc[1] = (f4){0, 0, 0, 0};
#pragma unroll
        for (int k4 = 0; k4 < 8; ++k4) acc[1] = mfma_h(*(const u4*)(rp + 64 * k4), bfr[k4], acc[1]);
#pragma unroll
        for (int i = 0; i < 9; ++i) {
            WR(i, pkcn(bh));
            if (i < 8) bh = cmul2(bh, z128);
        }
        acc[2] = (f4){0, 0, 0, 0};
#pragma unroll
        for (int k4 = 0; k4 < 8; ++k4) acc[2] = mfma_h(*(const u4*)(rp + 64 * k4), bfr[k4], acc[2]);
        float* pd = (float*)sc;
        *(f4*)(pd + 128 * (g >> 1) + 4 * (g & 1) + 8 * r16) = acc[0] * S1inv;
        if (g < 2) *(f4*)(pd + 256 + 64 * g + 4 * r16) = acc[1] * S1inv;
        else {
            *(f2*)(pd + 384 + 64 * (g - 2) + 2 * r16) = (f2){acc[1][0], acc[1][1]} * S1inv;
            *(f2*)(pd + 384 + 64 * (g - 2) + 32 + 2 * r16) = (f2){acc[1][2], acc[1][3]} * S1inv;
        }
        if (g < 3) {
#pragma unroll
            for (int e = 0; e < 4; ++e) pd[512 + 16 * (4 * g + e) + r16] = acc[2][e] * S1inv;
        }
        if (lane == 0) *(float*)(lds + SSM_BD + 4 * wave) = bd;
    }
    __syncthreads();
    float SKinv[4];
    {
        const float* bdp = (const float*)(lds + SSM_BD);
        float SKs[4];
#pragma unroll
        for (int c = 0; c < 4; ++c) {
            float bb = 2.f * (bdp[2 * c] + bdp[2 * c + 1]);
            int e0 = (int)((__float_as_uint(bb) >> 23) & 255u) - 126; e0 = e0 < -100 ? -100 : e0;
            SKinv[c] = __uint_as_float((unsigned)(127 - 15 + e0) << 23); SKs[c] = __uint_as_float((unsigned)(127 + 15 - e0) << 23);
        }
        const int c = tid >> 7, q = tid & 127;
        const float SK = c == 0 ? SKs[0] : c == 1 ? SKs[1] : c == 2 ? SKs[2] : SKs[3];
#pragma unroll
        for (int rep = 0; rep < 2; ++rep) {
            const int qq = q + 128 * rep;
            if (qq < 164) {
                const char* s0 = lds + SSM_KS + (2 * c) * KS_WAVE + qq * 16;
                f4 v = (*(const f4*)s0 + *(const f4*)(s0 + KS_WAVE)) * SK;
                *(u2*)(lds + SSM_KD + c * 1312 + qq * 8) = (u2){pk_f16(v[0], v[1]), pk_f16(v[2], v[3])};
            }
        }
    }
    if (tid < 128) { *(u2*)(lds + SSM_XC + (tid >> 5) * 16640 + ((tid >> 3) & 3) * 4160 + 8 * (tid & 7)) = (u2){0, 0}; }
    if (wave == 7) wait_flags((const gu64*)(ws + WS_FX) + (sb & (NREP - 1)) * 64 + (lane & 31), true, tag);
    __syncthreads();
    const u4 st = __builtin_amdgcn_raw_buffer_load_b128(rs, WS_ST + tid * 16, 0, 16);
    {
        const int xa_i = tid & 255;
        int idx[8];
#pragma unroll
        for (int j = 0; j < 8; j += 2) {
            int l0 = 2047 - (8 * xa_i + j), l1 = l0 - 1;
            unsigned bits = pk_bf16((float)l0, (float)l1);
            idx[j] = l0 < 128 ? l0 : (int)((bits & 0xffffu) - 0x4280u);
            idx[j + 1] = l1 < 128 ? l1 : (int)((bits >> 16) - 0x4280u);
        }
#pragma unroll
        for (int rep = 0; rep < 2; ++rep) {
            const int c = (tid >> 8) + 2 * rep;
            const uint16_t* kd = (const uint16_t*)(lds + SSM_KD + c * 1312);
            u4 o;
#pragma unroll
            for (int j = 0; j < 4; ++j) o[j] = (unsigned)kd[idx[2 * j]] | ((unsigned)kd[idx[2 * j + 1]] << 16);
            char* kr = lds + SSM_KR + c * 4608;
            *(u4*)(kr + 16 * xa_i) = o;
            if (xa_i < 32) *(u4*)(kr + 4096 + 16 * xa_i) = (u4){0, 0, 0, 0};
        }
    }
    {
        float xv[4][4];
#pragma unroll
        for (int e = 0; e < 4; ++e) {
            const unsigned sw_ = st[e];
            const float mb = lo_f(sw_), rinv = __builtin_amdgcn_rcpf(hi_f(sw_));
#pragma unroll
            for (int h = 0; h < 2; ++h) {
                const unsigned uw = ucol[e][h];
                unsigned a = pk_bf16(lo_f(uw) - mb, hi_f(uw) - mb);
                unsigned r = pk_bf16(lo_f(a) * rinv, hi_f(a) * rinv);
                xv[2 * h][e] = lo_f(r); xv[2 * h + 1][e] = hi_f(r);
            }
        }
#pragma unroll
        for (int cc = 0; cc < 4; ++cc) {
            const unsigned d0 = pk_f16(xv[cc][0], xv[cc][1]), d1 = pk_f16(xv[cc][2], xv[cc][3]);
            const unsigned n0 = __shfl_down(d0, 1), n1 = __shfl_down(d1, 1);
            const unsigned mid = __builtin_amdgcn_alignbyte(d1, d0, 2), e1 = __builtin_amdgcn_alignbyte(n0, d1, 2), e2 = __builtin_amdgcn_alignbyte(n1, n0, 2);
            char* xc = lds + SSM_XC + cc * 16640 + 8 * (tid + 8);
            *(u2*)(xc) = (u2){d0, d1};
            if (lane < 63) {
                *(u2*)(xc + 4160) = (u2){mid, e1};
                *(u2*)(xc + 2 * 4160) = (u2){d1, n0};
                *(u2*)(xc + 3 * 4160) = (u2){e1, e2};
            } else {
                *(unsigned*)(xc + 4160) = mid; *(uint16_t*)(xc + 4160 + 4) = (uint16_t)(d1 >> 16);
                *(unsigned*)(xc + 2 * 4160) = d1;
                *(uint16_t*)(xc + 3 * 4160) = (uint16_t)(d1 >> 16);
            }
            if (lane == 0) {
                *(uint16_t*)(xc + 4160 - 2) = (uint16_t)d0;
                *(unsigned*)(xc + 2 * 4160 - 4) = d0;
                *(uint16_t*)(xc + 3 * 4160 - 6) = (uint16_t)d0; *(unsigned*)(xc + 3 * 4160 - 4) = mid;
            }
        }
    }
    __syncthreads();
    f4 acc[8];
#pragma unroll
    for (int n = 0; n < 8; ++n) acc[n] = (f4){0, 0, 0, 0};
    {
        const int rh = wave & 1;
        const char* xc = lds + SSM_XC + ch * 16640 + ((r16 + 1) & 3) * 4160 + 8 * (10 - 2 * g + ((r16 + 1) >> 2)) + 64 * 4 * rh;
        const char* kr = lds + SSM_KR + ch * 4608 + 16 * (257 - 2 * r16 - g) + 64 * 4 * rh;
#pragma unroll
        for (int rr = 0; rr < 4; ++rr) {
            u4 af[8], bfv[8];
#pragma unroll
            for (int kp = 0; kp < 8; ++kp) {
                const char* p = xc + 64 * (8 * kp + rr);
                u2 a = *(const u2*)p, b = *(const u2*)(p + 8);
                af[kp] = (u4){a.x, a.y, b.x, b.y};
            }
#pragma unroll
            for (int j = 0; j < 8; ++j) bfv[j] = *(const u4*)(kr - 64 * (8 * j - rr));
#pragma unroll
            for (int n = 0; n < 8; ++n)
#pragma unroll
                for (int kp = 0; kp <= n; ++kp) acc[n] = mfma_h(af[kp], bfv[n - kp], acc[n]);
        }
    }
    {
        char* red = lds + SSM_RED;
#pragma unroll
        for (int n = 0; n < 8; ++n) *(f4*)(red + ((wave * 8 + n) * 64 + lane) * 16) = acc[n];
    }
    __syncthreads();
    {
        const char* red = lds + SSM_RED;
        const int t = 256 * wave + 16 * r16 + 4 * g;
        float s[4][4];
#pragma unroll
        for (int c = 0; c < 4; ++c) {
            f4 y = *(const f4*)(red + (((2 * c) * 8 + wave) * 64 + lane) * 16) + *(const f4*)(red + (((2 * c + 1) * 8 + wave) * 64 + lane) * 16);
            const u2 xx = *(const u2*)(lds + SSM_XC + c * 16640 + 2 * (t + 32));
            const unsigned dw_ = dsk4[c >> 1];
            const float ds = (c & 1) ? hi_f(dw_) : lo_f(dw_);
#pragma unroll
            for (int e = 0; e < 4; ++e) {
                const unsigned xw = xx[e >> 1];
                float x = (e & 1) ? hi_h(xw) : lo_h(xw);
                s[c][e] = rbf(y[e] * SKinv[c]) + rbf(x * ds);
            }
        }
        const unsigned soff = WS_S + sb * 16384 + t * 8;
        __builtin_amdgcn_raw_buffer_store_b128((u4){pk_bf16(s[0][0], s[1][0]), pk_bf16(s[2][0], s[3][0]), pk_bf16(s[0][1], s[1][1]), pk_bf16(s[2][1], s[3][1])}, rs, soff, 0, 16);
        __builtin_amdgcn_raw_buffer_store_b128((u4){pk_bf16(s[0][2], s[1][2]), pk_bf16(s[2][2], s[3][2]), pk_bf16(s[0][3], s[1][3]), pk_bf16(s[2][3], s[3][3])}, rs, soff + 16, 0, 16);
    }
    WAIT_VM(0);
    __syncthreads();
    if (tid >= 448 && tid < 448 + NREP) __hip_atomic_store((gu64*)(ws + WS_FS) + (tid - 448) * 128 + sb, tag, RLX_AGENT);
}

__global__ __launch_bounds__(512, 2) void fused_kernel(const uint16_t* __restrict__ u, const uint16_t* __restrict__ lar, const uint16_t* __restrict__ aim,
                                                       const uint16_t* __restrict__ cr, const uint16_t* __restrict__ ci,
                                                       const uint16_t* __restrict__ ldt, const uint16_t* __restrict__ dsk,
                                                       const uint16_t* __restrict__ w1, const uint16_t* __restrict__ w2,
                                                       char* ws, uint16_t* __restrict__ out) {
    extern __shared__ __attribute__((aligned(16))) char lds[];
    const int b = blockIdx.x;
    if (b < NFFN) ffn_role(u, w1, w2, ws, out, lds, b);
    else ssm_role(u, lar, aim, cr, ci, ldt, dsk, ws, lds, b - NFFN);
}

extern "C" void kernel_launch(void* const* d_in, const int* in_sizes, int n_in,
                              void* d_out, int out_size, void* d_ws, size_t ws_size,
                              hipStream_t stream) {
    (void)hipFuncSetAttribute((const void*)fused_kernel, hipFuncAttributeMaxDynamicSharedMemorySize, LDS_BYTES);
    fused_kernel<<<NFFN + NSSM, 512, LDS_BYTES, stream>>>((const uint16_t*)d_in[0], (const uint16_t*)d_in[1], (const uint16_t*)d_in[2], (const uint16_t*)d_in[3],
                                                  (const uint16_t*)d_in[4], (const uint16_t*)d_in[5], (const uint16_t*)d_in[6], (const uint16_t*)d_in[8],
                                                  (const uint16_t*)d_in[9], (char*)d_ws, (uint16_t*)d_out);
}
```
